```python
import torch

def _softmax(x, axis=-1):
    return torch.softmax(x, dim=axis)
def _gelu(x):
    return torch.nn.functional.gelu(x, approximate="tanh")
def _silu(x):
    return torch.nn.functional.silu(x)
def _sigmoid(x):
    return torch.sigmoid(x)
def _layernorm(x, axis=-1, eps=1e-5):
    m = x.mean(dim=axis, keepdim=True)
    v = x.var(dim=axis, keepdim=True, unbiased=False)
    return (x - m) / torch.sqrt(v + eps)
def _scatter_add(tgt, idx, src, axis=0):
    return tgt.clone().index_add_(axis, idx, src)
def _segment_sum(src, seg, n):
    out = torch.zeros((n,) + src.shape[1:], dtype=src.dtype)
    return out.index_add_(0, seg, src)
def _pad(x, pw):
    flat = tuple(v for pair in reversed(pw) for v in pair)
    return torch.nn.functional.pad(x, flat)

kernel_name = 'standalone__swiglu_ffn__sampling'

def setup_inputs():

    _w = (lambda a: (lambda t: t.to(torch.bfloat16) if t.is_floating_point() else t)(torch.as_tensor(a)))
    B, S, D, F = 32, 32, 1024, 3584
    return {
        "x": _w((lambda *s: torch.randn(*s, dtype=torch.bfloat16) * 0.05)(B, S, D)),
        "w_gate": _w((lambda *s: torch.randn(*s, dtype=torch.bfloat16) * 0.05)(D, F)),
        "w_up": _w((lambda *s: torch.randn(*s, dtype=torch.bfloat16) * 0.05)(D, F)),
        "w_down": _w((lambda *s: torch.randn(*s, dtype=torch.bfloat16) * 0.05)(F, D)),
    }

def reference(x, w_gate, w_up, w_down):
    g = _silu(x @ w_gate)
    u = x @ w_up
    return (g * u) @ w_down
```

```cpp
#include <hip/hip_runtime.h>
#include <hip/hip_bf16.h>

typedef __bf16 bf16x8 __attribute__((ext_vector_type(8)));
typedef __bf16 bf16x4 __attribute__((ext_vector_type(4)));
typedef __bf16 bf16x2 __attribute__((ext_vector_type(2)));
typedef float f32x4 __attribute__((ext_vector_type(4)));
typedef float f32x2 __attribute__((ext_vector_type(2)));
typedef unsigned u32x4 __attribute__((ext_vector_type(4)));
#define LAS __attribute__((address_space(3)))
#define GAS __attribute__((address_space(1)))
#define DI __device__ __forceinline__
#define SCHED() __builtin_amdgcn_sched_barrier(0)
#define CFENCE() asm volatile("" ::: "memory")
#define BAR() do { CFENCE(); __builtin_amdgcn_s_barrier(); CFENCE(); } while (0)
#define WAIT_VM(n) asm volatile("s_waitcnt vmcnt(%0)" ::"n"(n) : "memory")
#define WAIT_LGKM0() asm volatile("s_waitcnt lgkmcnt(0)" ::: "memory")

constexpr int MT = 1024, DM = 1024, FF = 3584;
constexpr int KS32 = FF / 32;
constexpr unsigned ST1 = 49152;
constexpr int NS1 = 3;
constexpr unsigned ST2 = 32768;
constexpr int NS2 = 4;
constexpr unsigned LDS_BYTES = 147456;
constexpr size_t WS_H = 0;
constexpr size_t WS_PART = (size_t)MT * FF * 2;
constexpr size_t WS_FLAG = WS_PART + (size_t)64 * 4 * 3 * 16384;

DI __amdgpu_buffer_rsrc_t mk_rsrc(const void* p, unsigned bytes) {
  return __builtin_amdgcn_make_buffer_rsrc((void*)p, 0, bytes, 0x00020000);
}
DI void dma16(__amdgpu_buffer_rsrc_t r, unsigned lds_off, unsigned voff, unsigned soff) {
  __builtin_amdgcn_raw_ptr_buffer_load_lds(r, (LAS void*)(size_t)lds_off, 16, voff, soff, 0, 0);
}
DI bf16x8 lds_b128(unsigned addr) { return *(LAS bf16x8*)(size_t)addr; }
DI bf16x8 lds_tr2(unsigned a0, unsigned a1) {
  bf16x4 lo = __builtin_amdgcn_ds_read_tr16_b64_v4bf16((LAS bf16x4*)(size_t)a0);
  bf16x4 hi = __builtin_amdgcn_ds_read_tr16_b64_v4bf16((LAS bf16x4*)(size_t)a1);
  return __builtin_shufflevector(lo, hi, 0, 1, 2, 3, 4, 5, 6, 7);
}
DI f32x4 mfma(bf16x8 a, bf16x8 b, f32x4 c) { return __builtin_amdgcn_mfma_f32_16x16x32_bf16(a, b, c, 0, 0, 0); }
DI unsigned pk_bf16(float lo, float hi) {
  f32x2 v = {lo, hi};
  bf16x2 b = __builtin_convertvector(v, bf16x2);
  return __builtin_bit_cast(unsigned, b);
}
DI float swiglu(float g, float u) {
  float e = __builtin_amdgcn_exp2f(g * -1.44269504088896f);
  return g * u * __builtin_amdgcn_rcpf(1.0f + e);
}

struct P1C { bf16x8 X[8]; bf16x8 Wa[4]; bf16x8 Wb[4]; f32x4 acc[8][4]; unsigned ax0, ax1, aw0, aw1; };

template <bool LAST>
DI void p1_tile(P1C& c, unsigned delta) {
#pragma unroll
  for (int m = 0; m < 8; ++m) {
#pragma unroll
    for (int n = 0; n < 4; ++n) c.acc[m][n] = mfma(c.Wa[n], c.X[m], c.acc[m][n]);
    c.X[m] = lds_b128(c.ax1 + m * 2048);
    if (m < 4) {
      unsigned a = ((m & 1) ? c.aw1 : c.aw0) + (m >> 1) * 16384 + 8192;
      c.Wb[((m >> 1) << 1) | (m & 1)] = lds_tr2(a, a + 1024);
    }
    SCHED();
  }
#pragma unroll
  for (int m = 0; m < 2; ++m) {
#pragma unroll
    for (int n = 0; n < 4; ++n) c.acc[m][n] = mfma(c.Wb[n], c.X[m], c.acc[m][n]);
    SCHED();
  }
  if (!LAST) {
    c.ax0 += delta; c.ax1 += delta; c.aw0 += delta; c.aw1 += delta;
    WAIT_LGKM0();
    BAR();
    SCHED();
    c.X[0] = lds_b128(c.ax0);
    c.X[1] = lds_b128(c.ax0 + 2048);
    SCHED();
  }
#pragma unroll
  for (int m = 2; m < 8; ++m) {
#pragma unroll
    for (int n = 0; n < 4; ++n) c.acc[m][n] = mfma(c.Wb[n], c.X[m], c.acc[m][n]);
    if (!LAST) {
      c.X[m] = lds_b128(c.ax0 + m * 2048);
      if (m < 6) {
        const int i = m - 2;
        unsigned a = ((i & 1) ? c.aw1 : c.aw0) + (i >> 1) * 16384;
        c.Wa[i] = lds_tr2(a, a + 1024);
      }
    }
    SCHED();
  }
}

__global__ __launch_bounds__(512, 2) void k_phase1(const __hip_bfloat16* __restrict__ x, const __hip_bfloat16* __restrict__ wg,
                                                   const __hip_bfloat16* __restrict__ wu, char* __restrict__ ws) {
  extern __shared__ __attribute__((aligned(16))) char smem[];
  const int bid = blockIdx.x, xcd = bid & 7, idx = bid >> 3;
  const int ksp = xcd & 3, tb = (xcd >> 2) * 4 + (idx >> 3), j = idx & 7;
  const int wave = __builtin_amdgcn_readfirstlane(threadIdx.x >> 6), l = threadIdx.x & 63;
  if (threadIdx.x < 64) ((unsigned*)(ws + WS_FLAG))[bid * 64 + threadIdx.x] = 0u;
  if (j == 7) return;
  const int fb = ksp * 7 + j;
  const unsigned L0 = (unsigned)(size_t)(LAS char*)smem;
  if (wave >= 4) {
    const int lw = wave - 4;
    __amdgpu_buffer_rsrc_t rx = mk_rsrc(x, MT * DM * 2), rg = mk_rsrc(wg, DM * FF * 2), ru = mk_rsrc(wu, DM * FF * 2);
    const int xr = 8 * lw + (l >> 3), xr15 = xr & 15;
    const unsigned sx = ((xr15 >> 1) & 3) | ((((xr15 >> 2) ^ (xr15 >> 3)) & 1) << 2);
    const unsigned vx = xr * 2048 + (((l & 7) ^ sx) << 4);
    const unsigned sw = (l >> 4) | (((lw >> 1) & 1) << 2);
    const unsigned vw = (4 * lw + (l >> 4)) * (FF * 2) + (((((l & 15) >> 1) ^ sw)) << 5) + ((l & 1) << 4);
    const unsigned sx0 = tb * 128 * 2048, sw0 = fb * 256;
    auto issue = [&](int kt, unsigned slot) {
      const unsigned lb = L0 + slot * ST1 + lw * 1024;
#pragma unroll
      for (int a = 0; a < 4; ++a) {
        dma16(rg, lb + 16384 + a * 4096, vw, sw0 + (kt * 64 + 16 * a) * (FF * 2));
        dma16(ru, lb + 32768 + a * 4096, vw, sw0 + (kt * 64 + 16 * a) * (FF * 2));
        dma16(rx, lb + a * 4096, vx, sx0 + a * 65536 + kt * 128);
      }
    };
    issue(0, 0); issue(1, 1); issue(2, 2);
    WAIT_VM(24); BAR();
    unsigned slot = 0;
    for (int t = 1; t < 14; ++t) {
      WAIT_VM(12); BAR();
      issue(t + 2, slot);
      slot = slot == 2 ? 0 : slot + 1;
    }
    WAIT_VM(12); BAR();
    WAIT_VM(0); BAR();
    return;
  }
  P1C c;
  const int g = l >> 4, q = (l & 15) >> 2, p = l & 3, r15 = l & 15;
  {
    const unsigned sx = ((r15 >> 1) & 3) | ((((r15 >> 2) ^ (r15 >> 3)) & 1) << 2);
    c.ax0 = L0 + r15 * 128 + ((g ^ sx) << 4);
    c.ax1 = L0 + r15 * 128 + (((4 + g) ^ sx) << 4);
    const unsigned sw = q | ((g & 1) << 2);
    c.aw0 = L0 + 16384 + (8 * g + q) * 256 + p * 8 + (((2 * wave) ^ sw) << 5);
    c.aw1 = L0 + 16384 + (8 * g + q) * 256 + p * 8 + (((2 * wave + 1) ^ sw) << 5);
  }
#pragma unroll
  for (int m = 0; m < 8; ++m)
#pragma unroll
    for (int n = 0; n < 4; ++n) c.acc[m][n] = (f32x4){0.f, 0.f, 0.f, 0.f};
  BAR();
#pragma unroll
  for (int i = 0; i < 4; ++i) {
    unsigned a = ((i & 1) ? c.aw1 : c.aw0) + (i >> 1) * 16384;
    c.Wa[i] = lds_tr2(a, a + 1024);
  }
#pragma unroll
  for (int m = 0; m < 8; ++m) c.X[m] = lds_b128(c.ax0 + m * 2048);
  SCHED();
  int slot = 0;
  for (int t = 0; t < 15; ++t) {
    const unsigned delta = slot == 2 ? (unsigned)(-2 * (int)ST1) : ST1;
    slot = slot == 2 ? 0 : slot + 1;
    p1_tile<false>(c, delta);
  }
  p1_tile<true>(c, 0);
  __amdgpu_buffer_rsrc_t rh = mk_rsrc(ws + WS_H, MT * FF * 2);
  const unsigned hs0 = (unsigned)((tb * 8) * KS32 + fb * 4 + wave) * 1024u;
#pragma unroll
  for (int m = 0; m < 8; ++m) {
    u32x4 o;
    o.x = pk_bf16(swiglu(c.acc[m][0][0], c.acc[m][2][0]), swiglu(c.acc[m][0][1], c.acc[m][2][1]));
    o.y = pk_bf16(swiglu(c.acc[m][0][2], c.acc[m][2][2]), swiglu(c.acc[m][0][3], c.acc[m][2][3]));
    o.z = pk_bf16(swiglu(c.acc[m][1][0], c.acc[m][3][0]), swiglu(c.acc[m][1][1], c.acc[m][3][1]));
    o.w = pk_bf16(swiglu(c.acc[m][1][2], c.acc[m][3][2]), swiglu(c.acc[m][1][3], c.acc[m][3][3]));
    __builtin_amdgcn_raw_buffer_store_b128(o, rh, l * 16, hs0 + m * (KS32 * 1024), 0);
  }
}

struct P2C { bf16x8 H[4]; bf16x8 Wa[4]; bf16x8 Wb[4]; f32x4 acc[4][4]; unsigned ah, aw[4]; };

template <bool LAST>
DI void p2_tile(P2C& c, unsigned delta) {
#pragma unroll
  for (int m = 0; m < 4; ++m) {
#pragma unroll
    for (int n = 0; n < 4; ++n) c.acc[m][n] = mfma(c.Wa[n], c.H[m], c.acc[m][n]);
    c.H[m] = lds_b128(c.ah + m * 2048 + 1024);
    if (m < 2) {
      c.Wb[2 * m] = lds_tr2(c.aw[2 * m] + 8192, c.aw[2 * m] + 8192 + 4096);
      c.Wb[2 * m + 1] = lds_tr2(c.aw[2 * m + 1] + 8192, c.aw[2 * m + 1] + 8192 + 4096);
    }
    SCHED();
  }
#pragma unroll
  for (int n = 0; n < 4; ++n) c.acc[0][n] = mfma(c.Wb[n], c.H[0], c.acc[0][n]);
  SCHED();
  if (!LAST) {
    c.ah += delta;
#pragma unroll
    for (int n = 0; n < 4; ++n) c.aw[n] += delta;
    WAIT_LGKM0();
    BAR();
    SCHED();
    c.H[0] = lds_b128(c.ah);
    c.Wa[0] = lds_tr2(c.aw[0], c.aw[0] + 4096);
    c.Wa[1] = lds_tr2(c.aw[1], c.aw[1] + 4096);
    SCHED();
  }
#pragma unroll
  for (int m = 1; m < 4; ++m) {
#pragma unroll
    for (int n = 0; n < 4; ++n) c.acc[m][n] = mfma(c.Wb[n], c.H[m], c.acc[m][n]);
    if (!LAST) {
      if (m == 1) {
        c.Wa[2] = lds_tr2(c.aw[2], c.aw[2] + 4096);
        c.Wa[3] = lds_tr2(c.aw[3], c.aw[3] + 4096);
      }
      c.H[m] = lds_b128(c.ah + m * 2048);
    }
    SCHED();
  }
}

__global__ __launch_bounds__(512, 2) void k_phase2(const __hip_bfloat16* __restrict__ wd, char* __restrict__ ws,
                                                   __hip_bfloat16* __restrict__ out) {
  extern __shared__ __attribute__((aligned(16))) char smem[];
  const int bid = blockIdx.x, xcd = bid & 7, idx = bid >> 3;
  const int ksp = xcd & 3, tb = (xcd >> 2) * 4 + (idx >> 3), cb = idx & 7;
  const int wave = __builtin_amdgcn_readfirstlane(threadIdx.x >> 6), l = threadIdx.x & 63;
  const unsigned L0 = (unsigned)(size_t)(LAS char*)smem;
  constexpr int NT = 14;
  if (wave >= 4) {
    const int lw = wave - 4;
    __amdgpu_buffer_rsrc_t rh = mk_rsrc(ws + WS_H, MT * FF * 2), rw = mk_rsrc(wd, FF * DM * 2);
    const unsigned vh = l * 16;
    const unsigned sw = 4 * (lw & 1) + (l >> 4);
    const unsigned vw = (4 * lw + (l >> 4)) * (DM * 2) + (((((l & 15) >> 1) ^ sw)) << 5) + ((l & 1) << 4);
    unsigned sh[4];
#pragma unroll
    for (int a = 0; a < 4; ++a) {
      const int tt = 4 * (a >> 1) + (((lw >> 1) + 2 * (a & 1) + ksp) & 3);
      sh[a] = (unsigned)((tb * 8 + tt) * KS32 + ksp * 28 + (lw & 1)) * 1024u;
    }
    const unsigned sw0 = cb * 256 + ksp * 896 * (DM * 2);
    auto issue = [&](int kt, unsigned slot) {
      const unsigned lb = L0 + slot * ST2 + lw * 1024;
#pragma unroll
      for (int a = 0; a < 4; ++a) {
        dma16(rw, lb + 16384 + a * 4096, vw, sw0 + (kt * 64 + 16 * a) * (DM * 2));
        dma16(rh, lb + a * 4096, vh, sh[a] + kt * 2048);
      }
    };
    issue(0, 0); issue(1, 1); issue(2, 2); issue(3, 3);
    WAIT_VM(24); BAR();
    unsigned slot = 0;
    for (int t = 1; t < NT - 3; ++t) {
      WAIT_VM(16); BAR();
      issue(t + 3, slot);
      slot = (slot + 1) & 3;
    }
    WAIT_VM(16); BAR();
    WAIT_VM(8); BAR();
    WAIT_VM(0); BAR();
    return;
  }
  P2C c;
  const int g = l >> 4, q = (l & 15) >> 2, p = l & 3, wm = wave >> 1, wn = wave & 1;
  {
    c.ah = L0 + wm * 8192 + l * 16;
    const unsigned sw = 4 * (g & 1) + q;
#pragma unroll
    for (int n = 0; n < 4; ++n) c.aw[n] = L0 + 16384 + (4 * g + q) * 256 + p * 8 + (((4 * wn + n) ^ sw) << 5);
  }
#pragma unroll
  for (int m = 0; m < 4; ++m)
#pragma unroll
    for (int n = 0; n < 4; ++n) c.acc[m][n] = (f32x4){0.f, 0.f, 0.f, 0.f};
  BAR();
#pragma unroll
  for (int n = 0; n < 4; ++n) c.Wa[n] = lds_tr2(c.aw[n], c.aw[n] + 4096);
#pragma unroll
  for (int m = 0; m < 4; ++m) c.H[m] = lds_b128(c.ah + m * 2048);
  SCHED();
  int slot = 0;
  for (int t = 0; t < NT - 1; ++t) {
    const unsigned delta = slot == NS2 - 1 ? (unsigned)(-(NS2 - 1) * (int)ST2) : ST2;
    slot = slot == NS2 - 1 ? 0 : slot + 1;
    p2_tile<false>(c, delta);
  }
  p2_tile<true>(c, 0);
  const int tile = tb * 8 + cb;
  __amdgpu_buffer_rsrc_t rp = mk_rsrc(ws + WS_PART, 64u * 4 * 3 * 16384);
#pragma unroll
  for (int mm = 1; mm < 4; ++mm) {
    const unsigned so = (unsigned)(((tile * 4 + ((ksp + mm) & 3)) * 3 + (mm - 1)) * 4 + wave) * 4096u;
#pragma unroll
    for (int n = 0; n < 4; ++n)
      __builtin_amdgcn_raw_buffer_store_b128(__builtin_bit_cast(u32x4, c.acc[mm][n]), rp, l * 16 + n * 1024, so, 16);
  }
  WAIT_VM(0);
  GAS unsigned* flags = (GAS unsigned*)(ws + WS_FLAG);
  if (l < 3) {
    const int mm = l + 1;
    __hip_atomic_store(flags + ((tile * 4 + ((ksp + mm) & 3)) * 4 + wave) * 16 + (mm - 1), 1u, __ATOMIC_RELAXED, __HIP_MEMORY_SCOPE_AGENT);
  }
  {
    GAS unsigned* f = flags + ((tile * 4 + ksp) * 4 + wave) * 16 + (l < 3 ? l : 0);
    for (unsigned spins = 0; spins < (1u << 20); ++spins) {
      const unsigned v = __hip_atomic_load(f, __ATOMIC_RELAXED, __HIP_MEMORY_SCOPE_AGENT);
      if (__all(v == 1u)) break;
      __builtin_amdgcn_s_sleep(1);
    }
    CFENCE();
  }
  f32x4 pr[3][4];
#pragma unroll
  for (int s = 0; s < 3; ++s) {
    const unsigned so = (unsigned)(((tile * 4 + ksp) * 3 + s) * 4 + wave) * 4096u;
#pragma unroll
    for (int n = 0; n < 4; ++n)
      pr[s][n] = __builtin_bit_cast(f32x4, __builtin_amdgcn_raw_buffer_load_b128(rp, l * 16 + n * 1024, so, 16));
  }
  unsigned pk[4][2];
#pragma unroll
  for (int n = 0; n < 4; ++n) {
    f32x4 v = ((c.acc[0][n] + pr[0][n]) + pr[1][n]) + pr[2][n];
    pk[n][0] = pk_bf16(v[0], v[1]);
    pk[n][1] = pk_bf16(v[2], v[3]);
  }
  __amdgpu_buffer_rsrc_t ro = mk_rsrc(out, MT * DM * 2);
  const unsigned row = tb * 128 + wm * 64 + ksp * 16 + (l & 15);
  const unsigned vo = row * (DM * 2) + (cb * 128 + wn * 64 + (g & 1) * 16 + (g >> 1) * 8) * 2;
#pragma unroll
  for (int h = 0; h < 2; ++h) {
    auto s0 = __builtin_amdgcn_permlane16_swap(pk[2 * h][0], pk[2 * h + 1][0], false, false);
    auto s1 = __builtin_amdgcn_permlane16_swap(pk[2 * h][1], pk[2 * h + 1][1], false, false);
    u32x4 o = {s0[0], s1[0], s0[1], s1[1]};
    __builtin_amdgcn_raw_buffer_store_b128(o, ro, vo + h * 64, 0, 0);
  }
}

extern "C" void kernel_launch(void* const* d_in, const int* in_sizes, int n_in, void* d_out, int out_size, void* d_ws,
                              size_t ws_size, hipStream_t stream) {
  const __hip_bfloat16* x = (const __hip_bfloat16*)d_in[0];
  const __hip_bfloat16* wg = (const __hip_bfloat16*)d_in[1];
  const __hip_bfloat16* wu = (const __hip_bfloat16*)d_in[2];
  const __hip_bfloat16* wd = (const __hip_bfloat16*)d_in[3];
  hipFuncSetAttribute((const void*)k_phase1, hipFuncAttributeMaxDynamicSharedMemorySize, LDS_BYTES);
  hipFuncSetAttribute((const void*)k_phase2, hipFuncAttributeMaxDynamicSharedMemorySize, LDS_BYTES);
  k_phase1<<<256, 512, NS1 * ST1, stream>>>(x, wg, wu, (char*)d_ws);
  k_phase2<<<256, 512, NS2 * ST2, stream>>>(wd, (char*)d_ws, (__hip_bfloat16*)d_out);
}
```

```cpp
#include <hip/hip_runtime.h>
#include <hip/hip_bf16.h>

typedef __bf16 bf16x8 __attribute__((ext_vector_type(8)));
typedef __bf16 bf16x4 __attribute__((ext_vector_type(4)));
typedef __bf16 bf16x2 __attribute__((ext_vector_type(2)));
typedef float f32x4 __attribute__((ext_vector_type(4)));
typedef float f32x2 __attribute__((ext_vector_type(2)));
typedef unsigned u32x4 __attribute__((ext_vector_type(4)));
typedef unsigned long long u64;
#define LAS __attribute__((address_space(3)))
#define GAS __attribute__((address_space(1)))
#define DI __device__ __forceinline__
#define SCHED() __builtin_amdgcn_sched_barrier(0)
#define CFENCE() asm volatile("" ::: "memory")
#define BAR() do { CFENCE(); __builtin_amdgcn_s_barrier(); CFENCE(); } while (0)
#define WAIT_VM(n) asm volatile("s_waitcnt vmcnt(%0)" ::"n"(n) : "memory")
#define WAIT_LGKM0() asm volatile("s_waitcnt lgkmcnt(0)" ::: "memory")
#define RLX_AGENT __ATOMIC_RELAXED, __HIP_MEMORY_SCOPE_AGENT

constexpr int MT = 1024, DM = 1024, FF = 3584;
constexpr int KS32 = FF / 32;
constexpr unsigned ST1 = 49152;
constexpr unsigned P1B = 16384;
constexpr unsigned ST2 = 32768;
constexpr unsigned LDS_BYTES = 163840;
constexpr int NPARK = 7;
constexpr int RD = 6;
constexpr size_t WS_H = 0;
constexpr size_t WS_FLAG = (size_t)MT * FF * 2;
constexpr size_t WS_EPOCH = WS_FLAG + 16384;

DI __amdgpu_buffer_rsrc_t mk_rsrc(const void* p, unsigned bytes) {
  return __builtin_amdgcn_make_buffer_rsrc((void*)p, 0, bytes, 0x00020000);
}
#define AUX_X 0
#define AUX_W 0
#define AUX_D 0
#define AUX_HS 16
#define AUX_HL 16
template <int AUX>
DI void dma16(__amdgpu_buffer_rsrc_t r, unsigned lds_off, unsigned voff, unsigned soff) {
  __builtin_amdgcn_raw_ptr_buffer_load_lds(r, (LAS void*)(size_t)lds_off, 16, voff, soff, 0, AUX);
}
template <int AUX>
DI void st16(u32x4 v, __amdgpu_buffer_rsrc_t r, unsigned voff, unsigned soff) {
  SCHED();
  __builtin_amdgcn_raw_buffer_store_b128(v, r, voff, soff, AUX);
  asm volatile("s_nop 1");
  SCHED();
}
DI bf16x8 lds_b128(unsigned addr) { return *(LAS bf16x8*)(size_t)addr; }
DI bf16x4 lds_tr(unsigned a) { return __builtin_amdgcn_ds_read_tr16_b64_v4bf16((LAS bf16x4*)(size_t)a); }
DI bf16x8 cat(bf16x4 lo, bf16x4 hi) { return __builtin_shufflevector(lo, hi, 0, 1, 2, 3, 4, 5, 6, 7); }
DI f32x4 mfma(bf16x8 a, bf16x8 b, f32x4 c) { return __builtin_amdgcn_mfma_f32_16x16x32_bf16(a, b, c, 0, 0, 0); }
DI unsigned pk_bf16(f32x2 v) { return __builtin_bit_cast(unsigned, __builtin_convertvector(v, bf16x2)); }
DI unsigned swiglu2(float g0, float g1, float u0, float u1) {
  const f32x2 g = {g0, g1}, u = {u0, u1};
  const f32x2 a = g * -1.44269504088896f;
  f32x2 e; e.x = __builtin_amdgcn_exp2f(a.x); e.y = __builtin_amdgcn_exp2f(a.y);
  const f32x2 d = e + 1.0f;
  f32x2 r; r.x = __builtin_amdgcn_rcpf(d.x); r.y = __builtin_amdgcn_rcpf(d.y);
  return pk_bf16((g * u) * r);
}

#define STAMP(i)

struct P1C { bf16x8 X[8]; bf16x4 Wl[2][4], Wh[2][4]; f32x4 acc[8][4]; unsigned ax0, ax1, aw0, aw1; };

template <bool LAST>
DI void p1_tile(P1C& c, unsigned delta) {
#pragma unroll
  for (int k = 0; k < 2; ++k) {
#pragma unroll
    for (int m = 0; m < 8; ++m) {
      if (k == 1 && m == 2) {
        WAIT_LGKM0();
        BAR();
        if (!LAST) { c.ax0 += delta; c.ax1 += delta; c.aw0 += delta; c.aw1 += delta; }
        SCHED();
      }
      const bool ld = !(LAST && k == 1);
      const unsigned ax = k ? c.ax0 : c.ax1;
      const int i = m - 2;
      const unsigned aw = ((i & 1) ? c.aw1 : c.aw0) + (i >> 1) * 16384 + (k ? 0 : 8192);
      c.acc[m][0] = mfma(cat(c.Wl[k][0], c.Wh[k][0]), c.X[m], c.acc[m][0]); SCHED();
      if (ld && m >= 1 + k) { c.X[m - 1 - k] = lds_b128(ax + (m - 1 - k) * 2048); SCHED(); }
      c.acc[m][1] = mfma(cat(c.Wl[k][1], c.Wh[k][1]), c.X[m], c.acc[m][1]); SCHED();
      if (ld && m >= 2 && m < 6) { c.Wl[1 - k][i & 3] = lds_tr(aw); SCHED(); }
      c.acc[m][2] = mfma(cat(c.Wl[k][2], c.Wh[k][2]), c.X[m], c.acc[m][2]); SCHED();
      if (ld && m >= 2 && m < 6) { c.Wh[1 - k][i & 3] = lds_tr(aw + 1024); SCHED(); }
      if (ld && m == 7 && k == 1) { c.X[6] = lds_b128(ax + 6 * 2048); SCHED(); }
      c.acc[m][3] = mfma(cat(c.Wl[k][3], c.Wh[k][3]), c.X[m], c.acc[m][3]); SCHED();
      if (ld && m == 7) { c.X[7] = lds_b128(ax + 7 * 2048); SCHED(); }
    }
  }
}

DI void wait_ready(GAS u64* f, u64 tag, int n) {
  const u64 need = n >= 64 ? ~0ull : ((1ull << n) - 1ull);
#pragma unroll 1
  for (unsigned spins = 0; spins < (1u << 22); ++spins) {
    const u64 v = __hip_atomic_load(f, RLX_AGENT);
    const u64 ok = __ballot(v == tag);
    if ((ok & need) == need) break;
    __builtin_amdgcn_s_sleep(2);
  }
  CFENCE();
}

__global__ __launch_bounds__(512, 2) void ffn_fused(const __hip_bfloat16* __restrict__ x, const __hip_bfloat16* __restrict__ wg,
                                                    const __hip_bfloat16* __restrict__ wu, const __hip_bfloat16* __restrict__ wd,
                                                    char* ws, __hip_bfloat16* __restrict__ out) {
  extern __shared__ __attribute__((aligned(16))) char smem[];
  const int bid = blockIdx.x, xcd = bid & 7, idx = bid >> 3;
  const int tb = (xcd >> 2) * 4 + (idx >> 3), j = idx & 7, fb = (xcd & 3) * 7 + j;
  const bool spare = j == 7;
  const int t2 = (xcd >> 1) * 4 + (idx >> 3), c2 = (xcd & 1) * 8 + (idx & 7);
  const int wave = __builtin_amdgcn_readfirstlane(threadIdx.x >> 6), l = threadIdx.x & 63;
  const unsigned L0 = (unsigned)(size_t)(LAS char*)smem;
  STAMP(0);

  if (wave >= 4) {
    const int lw = wave - 4;
    __amdgpu_buffer_rsrc_t rw = mk_rsrc(wd, FF * DM * 2);
    const unsigned vd = (l >> 3) * (DM * 2) + (((((l & 7) >> 1) ^ ((l >> 4) & 3))) << 5) + ((l & 1) << 4);
    const unsigned sd0 = c2 * 128 + lw * 64 * (DM * 2);
    const unsigned ld0 = L0 + lw * 8192;
    auto dma_w = [&](int i) {
#pragma unroll
      for (int a = 0; a < 8; ++a) dma16<AUX_D>(rw, ld0 + (i % 5) * ST2 + a * 1024, vd, sd0 + (i * 256 + a * 8) * (DM * 2));
    };
    u32x4 park[NPARK * 8];
    auto park_ld = [&](int e) {
      park[e] = __builtin_amdgcn_raw_buffer_load_b128(rw, vd, sd0 + ((5 + (e >> 3)) * 256 + (e & 7) * 8) * (DM * 2), AUX_D);
    };
    if (!spare) {
      __amdgpu_buffer_rsrc_t rx = mk_rsrc(x, MT * DM * 2), rg = mk_rsrc(wg, DM * FF * 2), ru = mk_rsrc(wu, DM * FF * 2);
      const int xr = 8 * lw + (l >> 3), xr15 = xr & 15;
      const unsigned sx = ((xr15 >> 1) & 3) | ((((xr15 >> 2) ^ (xr15 >> 3)) & 1) << 2);
      const unsigned vx = xr * 2048 + (((l & 7) ^ sx) << 4);
      const unsigned sw = (l >> 4) | (((lw >> 1) & 1) << 2);
      const unsigned vw = (4 * lw + (l >> 4)) * (FF * 2) + (((((l & 15) >> 1) ^ sw)) << 5) + ((l & 1) << 4);
      const unsigned sx0 = tb * 128 * 2048, sw0 = fb * 256;
      auto issue = [&](int kt, unsigned slot) {
        const unsigned lb = L0 + P1B + slot * ST1 + lw * 1024;
#pragma unroll
        for (int a = 0; a < 4; ++a) {
          dma16<AUX_W>(rg, lb + 16384 + a * 4096, vw, sw0 + (kt * 64 + 16 * a) * (FF * 2));
          dma16<AUX_W>(ru, lb + 32768 + a * 4096, vw, sw0 + (kt * 64 + 16 * a) * (FF * 2));
          dma16<AUX_X>(rx, lb + a * 4096, vx, sx0 + a * 65536 + kt * 128);
        }
      };
      issue(0, 2); issue(1, 0);
      WAIT_VM(12); BAR();
      issue(2, 1);
      unsigned slot = 2;
      for (int t = 1; t < 14; ++t) {
        WAIT_VM(12); BAR();
        issue(t + 2, slot);
        slot = slot == 2 ? 0 : slot + 1;
      }
#pragma unroll
      for (int e = 0; e < 4; ++e) park_ld(e);
      WAIT_VM(16); BAR();
      dma_w(0); dma_w(1);
      WAIT_VM(20); BAR();
      dma_w(2);
#pragma unroll
      for (int e = 4; e < 12; ++e) park_ld(e);
      BAR();
      dma_w(3); dma_w(4);
    } else {
      dma_w(0); dma_w(1); dma_w(2); dma_w(3); dma_w(4);
#pragma unroll
      for (int e = 0; e < 12; ++e) park_ld(e);
    }
    WAIT_VM(16);
#pragma unroll
    for (int e = 12; e < NPARK * 8; ++e) park_ld(e);
    STAMP(1);
    WAIT_VM(NPARK * 8 - 12);
    BAR();
#pragma unroll
    for (int i = 1; i < 14; ++i) {
      if (i == 12) WAIT_VM(8);
      if (i == 13) WAIT_VM(0);
      WAIT_LGKM0();
      BAR();
      const int n = i + 4;
      if (n < 5 + NPARK) {
#pragma unroll
        for (int a = 0; a < 8; ++a) *(LAS u32x4*)(size_t)(ld0 + (n % 5) * ST2 + a * 1024 + l * 16) = park[(n - 5) * 8 + a];
      } else if (n < 14) {
        dma_w(n);
      }
    }
    BAR();
    BAR();
    return;
  }

  GAS u64* flags = (GAS u64*)(ws + WS_FLAG);
  const u64 tag = __hip_atomic_load((GAS u64*)(ws + WS_EPOCH), RLX_AGENT) + 1ull;
  const int g = l >> 4, q = (l & 15) >> 2, p = l & 3, r15 = l & 15;
  __amdgpu_buffer_rsrc_t rh = mk_rsrc(ws + WS_H, MT * FF * 2);
  if (!spare) {
    P1C c;
    {
      const unsigned b = L0 + P1B + 2 * ST1;
      const unsigned sx = ((r15 >> 1) & 3) | ((((r15 >> 2) ^ (r15 >> 3)) & 1) << 2);
      c.ax0 = b + r15 * 128 + ((g ^ sx) << 4);
      c.ax1 = b + r15 * 128 + (((4 + g) ^ sx) << 4);
      const unsigned sw = q | ((g & 1) << 2);
      c.aw0 = b + 16384 + (8 * g + q) * 256 + p * 8 + (((2 * wave) ^ sw) << 5);
      c.aw1 = b + 16384 + (8 * g + q) * 256 + p * 8 + (((2 * wave + 1) ^ sw) << 5);
    }
#pragma unroll
    for (int m = 0; m < 8; ++m)
#pragma unroll
      for (int n = 0; n < 4; ++n) { c.acc[m][n] = (f32x4){0.f, 0.f, 0.f, 0.f}; asm volatile("" : "+v"(c.acc[m][n])); }
    __builtin_amdgcn_s_waitcnt(0xC07F);
    SCHED();
    BAR();
    STAMP(1);
#pragma unroll
    for (int i = 0; i < 4; ++i) {
      const unsigned a = ((i & 1) ? c.aw1 : c.aw0) + (i >> 1) * 16384;
      c.Wl[0][i] = lds_tr(a); c.Wh[0][i] = lds_tr(a + 1024);
      if (i == 0) c.X[0] = lds_b128(c.ax0);
    }
#pragma unroll
    for (int m = 1; m < 8; ++m) c.X[m] = lds_b128(c.ax0 + m * 2048);
    SCHED();
    int slot = 2;
    for (int t = 0; t < 15; ++t) {
      const unsigned delta = slot == 2 ? (unsigned)(-2 * (int)ST1) : ST1;
      slot = slot == 2 ? 0 : slot + 1;
      p1_tile<false>(c, delta);
    }
    p1_tile<true>(c, 0);
    STAMP(2);
    const unsigned hs0 = (unsigned)((tb * 8) * KS32 + fb * 4 + wave) * 1024u;
#pragma unroll
    for (int m = 0; m < 8; ++m) {
      u32x4 o;
      o.x = swiglu2(c.acc[m][0][0], c.acc[m][0][1], c.acc[m][2][0], c.acc[m][2][1]);
      o.y = swiglu2(c.acc[m][0][2], c.acc[m][0][3], c.acc[m][2][2], c.acc[m][2][3]);
      o.z = swiglu2(c.acc[m][1][0], c.acc[m][1][1], c.acc[m][3][0], c.acc[m][3][1]);
      o.w = swiglu2(c.acc[m][1][2], c.acc[m][1][3], c.acc[m][3][2], c.acc[m][3][3]);
      st16<AUX_HS>(o, rh, l * 16, hs0 + m * (KS32 * 1024));
    }
    STAMP(3);
    WAIT_VM(0);
    if (l == 0) __hip_atomic_store(flags + tb * 128 + fb * 4 + wave, tag, RLX_AGENT);
  } else {
    if (l == 0) __hip_atomic_store(flags + tb * 128 + 112 + (xcd & 3) * 4 + wave, tag, RLX_AGENT);
  }
  STAMP(4);

  {
    f32x4 acc[4][4];
    bf16x8 H[RD][4];
    bf16x4 Wl[2][4], Wh[2][4];
    unsigned aw[4];
    {
      const unsigned sw = 2 * (g & 1) + (q >> 1);
#pragma unroll
      for (int n = 0; n < 4; ++n) aw[n] = L0 + wave * 8192 + (4 * g + q) * 128 + p * 8 + ((n ^ sw) << 5);
    }
#pragma unroll
    for (int m = 0; m < 4; ++m)
#pragma unroll
      for (int n = 0; n < 4; ++n) { acc[m][n] = (f32x4){0.f, 0.f, 0.f, 0.f}; asm volatile("" : "+v"(acc[m][n])); }
    const int ls = l < 28 ? l : 0;
    GAS u64* fl = flags + (t2 >> 1) * 128 + 8 * (ls >> 1) + 2 * wave + (ls & 1);
    const unsigned hb = (unsigned)(t2 * 4 * KS32 + 2 * wave) * 1024u;
    auto ldh = [&](int s, int m) {
      H[s % RD][m] = __builtin_bit_cast(bf16x8, __builtin_amdgcn_raw_buffer_load_b128(
          rh, l * 16, hb + (unsigned)(m * KS32 + 8 * (s >> 1) + (s & 1)) * 1024u, AUX_HL));
    };
    wait_ready(fl, tag, 14);
    STAMP(5);
#pragma unroll
    for (int s = 0; s < RD; ++s)
#pragma unroll
      for (int m = 0; m < 4; ++m) ldh(s, m);
    SCHED();
    BAR();
#pragma unroll
    for (int n = 0; n < 4; ++n) { Wl[0][n] = lds_tr(aw[n]); Wh[0][n] = lds_tr(aw[n] + 2048); }
    SCHED();
#pragma unroll
    for (int s = 0; s < 28; ++s) {
      const int k = s & 1;
      if (s + RD == 14) wait_ready(fl, tag, 28);
      const unsigned nxt = k ? 0u : 4096u;
#pragma unroll
      for (int m = 0; m < 4; ++m) {
        if (k == 1 && m == 1 && s < 27) {
          WAIT_LGKM0(); BAR();
          const int in = (s + 1) >> 1;
          const unsigned delta = (in % 5) ? ST2 : (unsigned)(-4 * (int)ST2);
#pragma unroll
          for (int n = 0; n < 4; ++n) { aw[n] += delta; asm volatile("" : "+v"(aw[n])); }
          SCHED();
        }
        const int f0 = k ? (m - 1) : m;
#pragma unroll
        for (int n = 0; n < 4; ++n) {
          acc[m][n] = mfma(cat(Wl[k][n], Wh[k][n]), H[s % RD][m], acc[m][n]); SCHED();
          if (s < 27) {
            int e = -1;
            if (k == 0) { if (n < 2) e = 2 * f0 + n; }
            else if (m >= 1) { if (n < 3) e = 3 * f0 + n; if (e > 7) e = -1; }
            if (e >= 0) {
              const unsigned a = aw[e >> 1] + nxt + (e & 1) * 2048;
              if (e & 1) Wh[1 - k][e >> 1] = lds_tr(a); else Wl[1 - k][e >> 1] = lds_tr(a);
              SCHED();
            }
          }
        }
        if (s + RD < 28) { ldh(s + RD, m); SCHED(); }
      }
    }
    STAMP(6);
    BAR();
#pragma unroll
    for (int m = 0; m < 4; ++m)
#pragma unroll
      for (int n = 0; n < 4; ++n)
        *(LAS f32x4*)(size_t)(L0 + ((m * 4 + wave) * 4 + n) * 1024 + l * 16) = acc[m][n];
    WAIT_LGKM0();
    BAR();
    unsigned pk[4][2];
#pragma unroll
    for (int n = 0; n < 4; ++n) {
      f32x4 v = *(LAS f32x4*)(size_t)(L0 + ((wave * 4 + 0) * 4 + n) * 1024 + l * 16);
#pragma unroll
      for (int w = 1; w < 4; ++w) v += *(LAS f32x4*)(size_t)(L0 + ((wave * 4 + w) * 4 + n) * 1024 + l * 16);
      pk[n][0] = pk_bf16((f32x2){v[0], v[1]});
      pk[n][1] = pk_bf16((f32x2){v[2], v[3]});
    }
    __amdgpu_buffer_rsrc_t ro = mk_rsrc(out, MT * DM * 2);
    const unsigned row = t2 * 64 + wave * 16 + r15;
    const unsigned vo = row * (DM * 2) + (c2 * 64 + (g & 1) * 16 + (g >> 1) * 8) * 2;
#pragma unroll
    for (int h = 0; h < 2; ++h) {
      auto s0 = __builtin_amdgcn_permlane16_swap(pk[2 * h][0], pk[2 * h + 1][0], false, false);
      auto s1 = __builtin_amdgcn_permlane16_swap(pk[2 * h][1], pk[2 * h + 1][1], false, false);
      u32x4 o = {s0[0], s1[0], s0[1], s1[1]};
      st16<0>(o, ro, vo + h * 64, 0);
    }
  }
  if (bid == 0 && wave == 0) {
#pragma unroll 1
    for (unsigned spins = 0; spins < (1u << 22); ++spins) {
      bool ok = true;
#pragma unroll
      for (int i = 0; i < 16; ++i) ok &= __hip_atomic_load(flags + i * 64 + l, RLX_AGENT) == tag;
      if (__all(ok)) break;
      __builtin_amdgcn_s_sleep(2);
    }
    if (l == 0) __hip_atomic_store((GAS u64*)(ws + WS_EPOCH), tag, RLX_AGENT);
  }
  STAMP(7);
}

extern "C" void kernel_launch(void* const* d_in, const int* in_sizes, int n_in, void* d_out, int out_size, void* d_ws,
                              size_t ws_size, hipStream_t stream) {
  (void)hipFuncSetAttribute((const void*)ffn_fused, hipFuncAttributeMaxDynamicSharedMemorySize, LDS_BYTES);
  ffn_fused<<<256, 512, LDS_BYTES, stream>>>((const __hip_bfloat16*)d_in[0], (const __hip_bfloat16*)d_in[1],
                                             (const __hip_bfloat16*)d_in[2], (const __hip_bfloat16*)d_in[3], (char*)d_ws,
                                             (__hip_bfloat16*)d_out);
}
```

```cpp
#define PROBE 0
#include <hip/hip_runtime.h>
#include <hip/hip_bf16.h>

typedef __bf16 bf16x8 __attribute__((ext_vector_type(8)));
typedef __bf16 bf16x4 __attribute__((ext_vector_type(4)));
typedef __bf16 bf16x2 __attribute__((ext_vector_type(2)));
typedef float f32x4 __attribute__((ext_vector_type(4)));
typedef float f32x2 __attribute__((ext_vector_type(2)));
typedef unsigned u32x4 __attribute__((ext_vector_type(4)));
typedef unsigned long long u64;
#define LAS __attribute__((address_space(3)))
#define GAS __attribute__((address_space(1)))
#define DI __device__ __forceinline__
#define SCHED() __builtin_amdgcn_sched_barrier(0)
#define CFENCE() asm volatile("" ::: "memory")
#define BAR() do { CFENCE(); __builtin_amdgcn_s_barrier(); CFENCE(); } while (0)
#define WAIT_VM(n) asm volatile("s_waitcnt vmcnt(%0)" ::"n"(n) : "memory")
#define WAIT_LGKM0() asm volatile("s_waitcnt lgkmcnt(0)" ::: "memory")
#define RLX_AGENT __ATOMIC_RELAXED, __HIP_MEMORY_SCOPE_AGENT

constexpr int MT = 1024, DM = 1024, FF = 3584;
constexpr int KS32 = FF / 32;
constexpr unsigned ST1 = 49152;
constexpr unsigned P1B = 16384;
constexpr unsigned ST2 = 32768;
constexpr unsigned LDS_BYTES = 163840;
constexpr int NPARK = 7;
constexpr int RD = 6;
constexpr size_t WS_H = 0;
constexpr size_t WS_FLAG = (size_t)MT * FF * 2;
constexpr size_t WS_EPOCH = WS_FLAG + 16384;

DI __amdgpu_buffer_rsrc_t mk_rsrc(const void* p, unsigned bytes) {
  return __builtin_amdgcn_make_buffer_rsrc((void*)p, 0, bytes, 0x00020000);
}
#define AUX_X 0
#define AUX_W 0
#define AUX_D 0
#define AUX_HS 16
#define AUX_HL 16
template <int AUX>
DI void dma16(__amdgpu_buffer_rsrc_t r, unsigned lds_off, unsigned voff, unsigned soff) {
  __builtin_amdgcn_raw_ptr_buffer_load_lds(r, (LAS void*)(size_t)lds_off, 16, voff, soff, 0, AUX);
}
template <int AUX>
DI void st16(u32x4 v, __amdgpu_buffer_rsrc_t r, unsigned voff, unsigned soff) {
  SCHED();
  __builtin_amdgcn_raw_buffer_store_b128(v, r, voff, soff, AUX);
  asm volatile("s_nop 1");
  SCHED();
}
DI bf16x8 lds_b128(unsigned addr) { return *(LAS bf16x8*)(size_t)addr; }
DI bf16x4 lds_tr(unsigned a) { return __builtin_amdgcn_ds_read_tr16_b64_v4bf16((LAS bf16x4*)(size_t)a); }
DI bf16x8 cat(bf16x4 lo, bf16x4 hi) { return __builtin_shufflevector(lo, hi, 0, 1, 2, 3, 4, 5, 6, 7); }
DI f32x4 mfma(bf16x8 a, bf16x8 b, f32x4 c) { return __builtin_amdgcn_mfma_f32_16x16x32_bf16(a, b, c, 0, 0, 0); }
DI unsigned pk_bf16(f32x2 v) { return __builtin_bit_cast(unsigned, __builtin_convertvector(v, bf16x2)); }
DI unsigned swiglu2(float g0, float g1, float u0, float u1) {
  const f32x2 g = {g0, g1}, u = {u0, u1};
  const f32x2 a = g * -1.44269504088896f;
  f32x2 e; e.x = __builtin_amdgcn_exp2f(a.x); e.y = __builtin_amdgcn_exp2f(a.y);
  const f32x2 d = e + 1.0f;
  f32x2 r; r.x = __builtin_amdgcn_rcpf(d.x); r.y = __builtin_amdgcn_rcpf(d.y);
  return pk_bf16((g * u) * r);
}

DI u64 stamp_r() { u64 t; asm volatile("s_memrealtime %0\n\ts_waitcnt lgkmcnt(0)" : "=s"(t)::"memory"); return t; }
#define STAMP(i) do { if ((i) == 0) { pr_t0 = stamp_r(); pr_t = pr_t0; } if ((i) == PROBE) { SCHED(); pr_t = stamp_r(); __builtin_amdgcn_s_waitcnt(0xC07F); SCHED(); } \
  if ((i) == 7) { while (stamp_r() - pr_t0 < 6000ull + (pr_t - pr_t0)) __builtin_amdgcn_s_sleep(1); } } while (0)

struct P1C { bf16x8 X[8]; bf16x4 Wl[2][4], Wh[2][4]; f32x4 acc[8][4]; unsigned ax0, ax1, aw0, aw1; };

template <bool LAST>
DI void p1_tile(P1C& c, unsigned delta) {
#pragma unroll
  for (int k = 0; k < 2; ++k) {
#pragma unroll
    for (int m = 0; m < 8; ++m) {
      if (k == 1 && m == 2) {
        WAIT_LGKM0();
        BAR();
        if (!LAST) { c.ax0 += delta; c.ax1 += delta; c.aw0 += delta; c.aw1 += delta; }
        SCHED();
      }
      const bool ld = !(LAST && k == 1);
      const unsigned ax = k ? c.ax0 : c.ax1;
      const int i = m - 2;
      const unsigned aw = ((i & 1) ? c.aw1 : c.aw0) + (i >> 1) * 16384 + (k ? 0 : 8192);
      c.acc[m][0] = mfma(cat(c.Wl[k][0], c.Wh[k][0]), c.X[m], c.acc[m][0]); SCHED();
      if (ld && m >= 1 + k) { c.X[m - 1 - k] = lds_b128(ax + (m - 1 - k) * 2048); SCHED(); }
      c.acc[m][1] = mfma(cat(c.Wl[k][1], c.Wh[k][1]), c.X[m], c.acc[m][1]); SCHED();
      if (ld && m >= 2 && m < 6) { c.Wl[1 - k][i & 3] = lds_tr(aw); SCHED(); }
      c.acc[m][2] = mfma(cat(c.Wl[k][2], c.Wh[k][2]), c.X[m], c.acc[m][2]); SCHED();
      if (ld && m >= 2 && m < 6) { c.Wh[1 - k][i & 3] = lds_tr(aw + 1024); SCHED(); }
      if (ld && m == 7 && k == 1) { c.X[6] = lds_b128(ax + 6 * 2048); SCHED(); }
      c.acc[m][3] = mfma(cat(c.Wl[k][3], c.Wh[k][3]), c.X[m], c.acc[m][3]); SCHED();
      if (ld && m == 7) { c.X[7] = lds_b128(ax + 7 * 2048); SCHED(); }
    }
  }
}

DI void wait_ready(GAS u64* f, u64 tag, int n) {
  const u64 need = n >= 64 ? ~0ull : ((1ull << n) - 1ull);
#pragma unroll 1
  for (unsigned spins = 0; spins < (1u << 22); ++spins) {
    const u64 v = __hip_atomic_load(f, RLX_AGENT);
    const u64 ok = __ballot(v == tag);
    if ((ok & need) == need) break;
    __builtin_amdgcn_s_sleep(2);
  }
  CFENCE();
}

__global__ __launch_bounds__(512, 2) void ffn_fused(const __hip_bfloat16* __restrict__ x, const __hip_bfloat16* __restrict__ wg,
                                                    const __hip_bfloat16* __restrict__ wu, const __hip_bfloat16* __restrict__ wd,
                                                    char* ws, __hip_bfloat16* __restrict__ out) {
  extern __shared__ __attribute__((aligned(16))) char smem[];
  const int bid = blockIdx.x, xcd = bid & 7, idx = bid >> 3;
  const int tb = (xcd >> 2) * 4 + (idx >> 3), j = idx & 7, fb = (xcd & 3) * 7 + j;
  const bool spare = j == 7;
  const int t2 = (xcd >> 1) * 4 + (idx >> 3), c2 = (xcd & 1) * 8 + (idx & 7);
  const int wave = __builtin_amdgcn_readfirstlane(threadIdx.x >> 6), l = threadIdx.x & 63;
  const unsigned L0 = (unsigned)(size_t)(LAS char*)smem;
  u64 pr_t0, pr_t;
  STAMP(0);

  if (wave >= 4) {
    const int lw = wave - 4;
    __amdgpu_buffer_rsrc_t rw = mk_rsrc(wd, FF * DM * 2);
    const unsigned vd = (l >> 3) * (DM * 2) + (((((l & 7) >> 1) ^ ((l >> 4) & 3))) << 5) + ((l & 1) << 4);
    const unsigned sd0 = c2 * 128 + lw * 64 * (DM * 2);
    const unsigned ld0 = L0 + lw * 8192;
    auto dma_w = [&](int i) {
#pragma unroll
      for (int a = 0; a < 8; ++a) dma16<AUX_D>(rw, ld0 + (i % 5) * ST2 + a * 1024, vd, sd0 + (i * 256 + a * 8) * (DM * 2));
    };
    u32x4 park[NPARK * 8];
    auto park_ld = [&](int e) {
      park[e] = __builtin_amdgcn_raw_buffer_load_b128(rw, vd, sd0 + ((5 + (e >> 3)) * 256 + (e & 7) * 8) * (DM * 2), AUX_D);
    };
    if (!spare) {
      __amdgpu_buffer_rsrc_t rx = mk_rsrc(x, MT * DM * 2), rg = mk_rsrc(wg, DM * FF * 2), ru = mk_rsrc(wu, DM * FF * 2);
      const int xr = 8 * lw + (l >> 3), xr15 = xr & 15;
      const unsigned sx = ((xr15 >> 1) & 3) | ((((xr15 >> 2) ^ (xr15 >> 3)) & 1) << 2);
      const unsigned vx = xr * 2048 + (((l & 7) ^ sx) << 4);
      const unsigned sw = (l >> 4) | (((lw >> 1) & 1) << 2);
      const unsigned vw = (4 * lw + (l >> 4)) * (FF * 2) + (((((l & 15) >> 1) ^ sw)) << 5) + ((l & 1) << 4);
      const unsigned sx0 = tb * 128 * 2048, sw0 = fb * 256;
      auto issue = [&](int kt, unsigned slot) {
        const unsigned lb = L0 + P1B + slot * ST1 + lw * 1024;
#pragma unroll
        for (int a = 0; a < 4; ++a) {
          dma16<AUX_W>(rg, lb + 16384 + a * 4096, vw, sw0 + (kt * 64 + 16 * a) * (FF * 2));
          dma16<AUX_W>(ru, lb + 32768 + a * 4096, vw, sw0 + (kt * 64 + 16 * a) * (FF * 2));
          dma16<AUX_X>(rx, lb + a * 4096, vx, sx0 + a * 65536 + kt * 128);
        }
      };
      issue(0, 2); issue(1, 0);
      WAIT_VM(12); BAR();
      issue(2, 1);
      unsigned slot = 2;
      for (int t = 1; t < 14; ++t) {
        WAIT_VM(12); BAR();
        issue(t + 2, slot);
        slot = slot == 2 ? 0 : slot + 1;
      }
#pragma unroll
      for (int e = 0; e < 4; ++e) park_ld(e);
      WAIT_VM(16); BAR();
      dma_w(0); dma_w(1);
      WAIT_VM(20); BAR();
      dma_w(2);
#pragma unroll
      for (int e = 4; e < 12; ++e) park_ld(e);
      BAR();
      dma_w(3); dma_w(4);
    } else {
      dma_w(0); dma_w(1); dma_w(2); dma_w(3); dma_w(4);
#pragma unroll
      for (int e = 0; e < 12; ++e) park_ld(e);
    }
    WAIT_VM(16);
#pragma unroll
    for (int e = 12; e < NPARK * 8; ++e) park_ld(e);
    STAMP(1);
    WAIT_VM(NPARK * 8 - 12);
    BAR();
#pragma unroll
    for (int i = 1; i < 14; ++i) {
      if (i == 12) WAIT_VM(8);
      if (i == 13) WAIT_VM(0);
      WAIT_LGKM0();
      BAR();
      const int n = i + 4;
      if (n < 5 + NPARK) {
#pragma unroll
        for (int a = 0; a < 8; ++a) *(LAS u32x4*)(size_t)(ld0 + (n % 5) * ST2 + a * 1024 + l * 16) = park[(n - 5) * 8 + a];
      } else if (n < 14) {
        dma_w(n);
      }
    }
    BAR();
    BAR();
    return;
  }

  GAS u64* flags = (GAS u64*)(ws + WS_FLAG);
  const u64 tag = __hip_atomic_load((GAS u64*)(ws + WS_EPOCH), RLX_AGENT) + 1ull;
  const int g = l >> 4, q = (l & 15) >> 2, p = l & 3, r15 = l & 15;
  __amdgpu_buffer_rsrc_t rh = mk_rsrc(ws + WS_H, MT * FF * 2);
  if (!spare) {
    P1C c;
    {
      const unsigned b = L0 + P1B + 2 * ST1;
      const unsigned sx = ((r15 >> 1) & 3) | ((((r15 >> 2) ^ (r15 >> 3)) & 1) << 2);
      c.ax0 = b + r15 * 128 + ((g ^ sx) << 4);
      c.ax1 = b + r15 * 128 + (((4 + g) ^ sx) << 4);
      const unsigned sw = q | ((g & 1) << 2);
      c.aw0 = b + 16384 + (8 * g + q) * 256 + p * 8 + (((2 * wave) ^ sw) << 5);
      c.aw1 = b + 16384 + (8 * g + q) * 256 + p * 8 + (((2 * wave + 1) ^ sw) << 5);
    }
#pragma unroll
    for (int m = 0; m < 8; ++m)
#pragma unroll
      for (int n = 0; n < 4; ++n) { c.acc[m][n] = (f32x4){0.f, 0.f, 0.f, 0.f}; asm volatile("" : "+v"(c.acc[m][n])); }
    __builtin_amdgcn_s_waitcnt(0xC07F);
    SCHED();
    BAR();
    STAMP(1);
#pragma unroll
    for (int i = 0; i < 4; ++i) {
      const unsigned a = ((i & 1) ? c.aw1 : c.aw0) + (i >> 1) * 16384;
      c.Wl[0][i] = lds_tr(a); c.Wh[0][i] = lds_tr(a + 1024);
      if (i == 0) c.X[0] = lds_b128(c.ax0);
    }
#pragma unroll
    for (int m = 1; m < 8; ++m) c.X[m] = lds_b128(c.ax0 + m * 2048);
    SCHED();
    int slot = 2;
    for (int t = 0; t < 15; ++t) {
      const unsigned delta = slot == 2 ? (unsigned)(-2 * (int)ST1) : ST1;
      slot = slot == 2 ? 0 : slot + 1;
      p1_tile<false>(c, delta);
    }
    p1_tile<true>(c, 0);
    STAMP(2);
    const unsigned hs0 = (unsigned)((tb * 8) * KS32 + fb * 4 + wave) * 1024u;
#pragma unroll
    for (int m = 0; m < 8; ++m) {
      u32x4 o;
      o.x = swiglu2(c.acc[m][0][0], c.acc[m][0][1], c.acc[m][2][0], c.acc[m][2][1]);
      o.y = swiglu2(c.acc[m][0][2], c.acc[m][0][3], c.acc[m][2][2], c.acc[m][2][3]);
      o.z = swiglu2(c.acc[m][1][0], c.acc[m][1][1], c.acc[m][3][0], c.acc[m][3][1]);
      o.w = swiglu2(c.acc[m][1][2], c.acc[m][1][3], c.acc[m][3][2], c.acc[m][3][3]);
      st16<AUX_HS>(o, rh, l * 16, hs0 + m * (KS32 * 1024));
    }
    STAMP(3);
    WAIT_VM(0);
    if (l == 0) __hip_atomic_store(flags + tb * 128 + fb * 4 + wave, tag, RLX_AGENT);
  } else {
    if (l == 0) __hip_atomic_store(flags + tb * 128 + 112 + (xcd & 3) * 4 + wave, tag, RLX_AGENT);
  }
  STAMP(4);

  {
    f32x4 acc[4][4];
    bf16x8 H[RD][4];
    bf16x4 Wl[2][4], Wh[2][4];
    unsigned aw[4];
    {
      const unsigned sw = 2 * (g & 1) + (q >> 1);
#pragma unroll
      for (int n = 0; n < 4; ++n) aw[n] = L0 + wave * 8192 + (4 * g + q) * 128 + p * 8 + ((n ^ sw) << 5);
    }
#pragma unroll
    for (int m = 0; m < 4; ++m)
#pragma unroll
      for (int n = 0; n < 4; ++n) { acc[m][n] = (f32x4){0.f, 0.f, 0.f, 0.f}; asm volatile("" : "+v"(acc[m][n])); }
    const int ls = l < 28 ? l : 0;
    GAS u64* fl = flags + (t2 >> 1) * 128 + 8 * (ls >> 1) + 2 * wave + (ls & 1);
    const unsigned hb = (unsigned)(t2 * 4 * KS32 + 2 * wave) * 1024u;
    auto ldh = [&](int s, int m) {
      H[s % RD][m] = __builtin_bit_cast(bf16x8, __builtin_amdgcn_raw_buffer_load_b128(
          rh, l * 16, hb + (unsigned)(m * KS32 + 8 * (s >> 1) + (s & 1)) * 1024u, AUX_HL));
    };
    wait_ready(fl, tag, 14);
    STAMP(5);
#pragma unroll
    for (int s = 0; s < RD; ++s)
#pragma unroll
      for (int m = 0; m < 4; ++m) ldh(s, m);
    SCHED();
    BAR();
#pragma unroll
    for (int n = 0; n < 4; ++n) { Wl[0][n] = lds_tr(aw[n]); Wh[0][n] = lds_tr(aw[n] + 2048); }
    SCHED();
#pragma unroll
    for (int s = 0; s < 28; ++s) {
      const int k = s & 1;
      if (s + RD == 14) wait_ready(fl, tag, 28);
      const unsigned nxt = k ? 0u : 4096u;
#pragma unroll
      for (int m = 0; m < 4; ++m) {
        if (k == 1 && m == 1 && s < 27) {
          WAIT_LGKM0(); BAR();
          const int in = (s + 1) >> 1;
          const unsigned delta = (in % 5) ? ST2 : (unsigned)(-4 * (int)ST2);
#pragma unroll
          for (int n = 0; n < 4; ++n) { aw[n] += delta; asm volatile("" : "+v"(aw[n])); }
          SCHED();
        }
        const int f0 = k ? (m - 1) : m;
#pragma unroll
        for (int n = 0; n < 4; ++n) {
          acc[m][n] = mfma(cat(Wl[k][n], Wh[k][n]), H[s % RD][m], acc[m][n]); SCHED();
          if (s < 27) {
            int e = -1;
            if (k == 0) { if (n < 2) e = 2 * f0 + n; }
            else if (m >= 1) { if (n < 3) e = 3 * f0 + n; if (e > 7) e = -1; }
            if (e >= 0) {
              const unsigned a = aw[e >> 1] + nxt + (e & 1) * 2048;
              if (e & 1) Wh[1 - k][e >> 1] = lds_tr(a); else Wl[1 - k][e >> 1] = lds_tr(a);
              SCHED();
            }
          }
        }
        if (s + RD < 28) { ldh(s + RD, m); SCHED(); }
      }
    }
    STAMP(6);
    BAR();
#pragma unroll
    for (int m = 0; m < 4; ++m)
#pragma unroll
      for (int n = 0; n < 4; ++n)
        *(LAS f32x4*)(size_t)(L0 + ((m * 4 + wave) * 4 + n) * 1024 + l * 16) = acc[m][n];
    WAIT_LGKM0();
    BAR();
    unsigned pk[4][2];
#pragma unroll
    for (int n = 0; n < 4; ++n) {
      f32x4 v = *(LAS f32x4*)(size_t)(L0 + ((wave * 4 + 0) * 4 + n) * 1024 + l * 16);
#pragma unroll
      for (int w = 1; w < 4; ++w) v += *(LAS f32x4*)(size_t)(L0 + ((wave * 4 + w) * 4 + n) * 1024 + l * 16);
      pk[n][0] = pk_bf16((f32x2){v[0], v[1]});
      pk[n][1] = pk_bf16((f32x2){v[2], v[3]});
    }
    __amdgpu_buffer_rsrc_t ro = mk_rsrc(out, MT * DM * 2);
    const unsigned row = t2 * 64 + wave * 16 + r15;
    const unsigned vo = row * (DM * 2) + (c2 * 64 + (g & 1) * 16 + (g >> 1) * 8) * 2;
#pragma unroll
    for (int h = 0; h < 2; ++h) {
      auto s0 = __builtin_amdgcn_permlane16_swap(pk[2 * h][0], pk[2 * h + 1][0], false, false);
      auto s1 = __builtin_amdgcn_permlane16_swap(pk[2 * h][1], pk[2 * h + 1][1], false, false);
      u32x4 o = {s0[0], s1[0], s0[1], s1[1]};
      st16<0>(o, ro, vo + h * 64, 0);
    }
  }
  if (bid == 0 && wave == 0) {
#pragma unroll 1
    for (unsigned spins = 0; spins < (1u << 22); ++spins) {
      bool ok = true;
#pragma unroll
      for (int i = 0; i < 16; ++i) ok &= __hip_atomic_load(flags + i * 64 + l, RLX_AGENT) == tag;
      if (__all(ok)) break;
      __builtin_amdgcn_s_sleep(2);
    }
    if (l == 0) __hip_atomic_store((GAS u64*)(ws + WS_EPOCH), tag, RLX_AGENT);
  }
  STAMP(7);
}

extern "C" void kernel_launch(void* const* d_in, const int* in_sizes, int n_in, void* d_out, int out_size, void* d_ws,
                              size_t ws_size, hipStream_t stream) {
  (void)hipFuncSetAttribute((const void*)ffn_fused, hipFuncAttributeMaxDynamicSharedMemorySize, LDS_BYTES);
  ffn_fused<<<256, 512, LDS_BYTES, stream>>>((const __hip_bfloat16*)d_in[0], (const __hip_bfloat16*)d_in[1],
                                             (const __hip_bfloat16*)d_in[2], (const __hip_bfloat16*)d_in[3], (char*)d_ws,
                                             (__hip_bfloat16*)d_out);
}
```

```cpp
#include <hip/hip_runtime.h>
#include <hip/hip_bf16.h>

typedef __bf16 bf16x8 __attribute__((ext_vector_type(8)));
typedef __bf16 bf16x4 __attribute__((ext_vector_type(4)));
typedef __bf16 bf16x2 __attribute__((ext_vector_type(2)));
typedef float f32x4 __attribute__((ext_vector_type(4)));
typedef float f32x2 __attribute__((ext_vector_type(2)));
typedef unsigned u32x4 __attribute__((ext_vector_type(4)));
typedef unsigned long long u64;
typedef u64 u64x4 __attribute__((ext_vector_type(4)));
typedef u64 u64x2 __attribute__((ext_vector_type(2)));
#define LAS __attribute__((address_space(3)))
#define GAS __attribute__((address_space(1)))
#define DI __device__ __forceinline__
#define SCHED() __builtin_amdgcn_sched_barrier(0)
#define CFENCE() asm volatile("" ::: "memory")
#define BAR() do { CFENCE(); __builtin_amdgcn_s_barrier(); CFENCE(); } while (0)
#define WAIT_VM(n) asm volatile("s_waitcnt vmcnt(%0)" ::"n"(n) : "memory")
#define VIS_LGKM0() __builtin_amdgcn_s_waitcnt(0xC07F)
#define VIS_VM0() __builtin_amdgcn_s_waitcnt(0x0F70)
#define RLX_AGENT __ATOMIC_RELAXED, __HIP_MEMORY_SCOPE_AGENT

constexpr int MT = 1024, DM = 1024, FF = 3584;
constexpr int KS32 = FF / 32;
constexpr unsigned ST1 = 49152;
constexpr unsigned P1B = 16384;
constexpr unsigned SL2 = 16384;
constexpr unsigned WB2 = 4 * SL2;
constexpr unsigned LDS_BYTES = 163840;
constexpr unsigned SPIN_CAP = 1u << 22;
constexpr unsigned CODE_BYTES = 15360;
constexpr int SC1 = 16;
constexpr size_t WS_H = 0;
constexpr size_t WS_PART = (size_t)MT * FF * 2;
constexpr size_t WS_FLAG = WS_PART + (size_t)64 * 4 * 3 * 8192;
constexpr size_t WS_XFLAG = WS_FLAG + 32768;
constexpr size_t WS_EPOCH = WS_XFLAG + 65536;

DI __amdgpu_buffer_rsrc_t mk_rsrc(const void* p, unsigned bytes) {
  return __builtin_amdgcn_make_buffer_rsrc((void*)p, 0, bytes, 0x00020000);
}
template <int AUX, int IMM = 0>
DI void dma16(__amdgpu_buffer_rsrc_t r, unsigned lds_off, unsigned voff, unsigned soff) {
  __builtin_amdgcn_raw_ptr_buffer_load_lds(r, (LAS void*)(size_t)lds_off, 16, voff, soff, IMM, AUX);
}
DI void touch(__amdgpu_buffer_rsrc_t r, unsigned lds_off, unsigned voff, unsigned soff) {
  __builtin_amdgcn_raw_ptr_buffer_load_lds(r, (LAS void*)(size_t)lds_off, 4, voff, soff, 0, 0);
}
DI void st16(u32x4 v, __amdgpu_buffer_rsrc_t r, unsigned voff, unsigned soff) {
  SCHED();
  __builtin_amdgcn_raw_buffer_store_b128(v, r, voff, soff, SC1);
  asm volatile("s_nop 1");
  SCHED();
}
DI bf16x8 lds_b128(unsigned addr) { return *(LAS bf16x8*)(size_t)addr; }
DI bf16x4 lds_tr(unsigned a) { return __builtin_amdgcn_ds_read_tr16_b64_v4bf16((LAS bf16x4*)(size_t)a); }
DI bf16x8 cat(bf16x4 lo, bf16x4 hi) { return __builtin_shufflevector(lo, hi, 0, 1, 2, 3, 4, 5, 6, 7); }
DI f32x4 mfma(bf16x8 a, bf16x8 b, f32x4 c) { return __builtin_amdgcn_mfma_f32_16x16x32_bf16(a, b, c, 0, 0, 0); }
DI unsigned pk_bf16(float lo, float hi) { return __builtin_bit_cast(unsigned, __builtin_convertvector((f32x2){lo, hi}, bf16x2)); }

struct P1C { bf16x8 X[8]; bf16x4 Wl[2][4], Wh[2][4]; f32x4 acc[8][4]; unsigned ax0, ax1, aw0, aw1; };

DI void p1_tile(P1C& c, int& slot) {
  unsigned delta = 0;
#pragma unroll
  for (int k = 0; k < 2; ++k) {
#pragma unroll
    for (int m = 0; m < 8; ++m) {
      if (k == 1 && m == 2) { VIS_LGKM0(); BAR(); SCHED(); }
      const unsigned ax = k ? c.ax0 : c.ax1;
      const int i = m - 2;
      const unsigned aw = ((i & 1) ? c.aw1 : c.aw0) + (i >> 1) * 16384 + (k ? 0 : 8192);
      c.acc[m][0] = mfma(cat(c.Wl[k][0], c.Wh[k][0]), c.X[m], c.acc[m][0]); SCHED();
      if (m >= 1 + k) { c.X[m - 1 - k] = lds_b128(ax + (m - 1 - k) * 2048); SCHED(); }
      if (k == 1 && m == 0) { c.ax1 += delta; asm volatile("" : "+v"(c.ax1)); SCHED(); }
      c.acc[m][1] = mfma(cat(c.Wl[k][1], c.Wh[k][1]), c.X[m], c.acc[m][1]); SCHED();
      if (m >= 2 && m < 6) { c.Wl[1 - k][i & 3] = lds_tr(aw); SCHED(); }
      if (k == 0 && m == 1) {
        asm volatile("" : "+s"(slot));
        delta = slot == 2 ? (unsigned)(-2 * (int)ST1) : ST1;
        asm volatile("" : "+s"(delta));
        SCHED();
      }
      if (k == 0 && m == 6) { c.ax0 += delta; asm volatile("" : "+v"(c.ax0)); SCHED(); }
      if (k == 0 && m == 7) { c.aw1 += delta; asm volatile("" : "+v"(c.aw1)); SCHED(); }
      c.acc[m][2] = mfma(cat(c.Wl[k][2], c.Wh[k][2]), c.X[m], c.acc[m][2]); SCHED();
      if (m >= 2 && m < 6) { c.Wh[1 - k][i & 3] = lds_tr(aw + 1024); SCHED(); }
      if (m == 7 && k == 1) { c.X[6] = lds_b128(ax + 6 * 2048); SCHED(); }
      if (k == 0 && m == 1) { slot = slot == 2 ? 0 : slot + 1; asm volatile("" : "+s"(slot)); SCHED(); }
      if (k == 0 && m == 6) { c.aw0 += delta; asm volatile("" : "+v"(c.aw0)); SCHED(); }
      c.acc[m][3] = mfma(cat(c.Wl[k][3], c.Wh[k][3]), c.X[m], c.acc[m][3]); SCHED();
      if (m == 7) { c.X[7] = lds_b128(ax + 7 * 2048); SCHED(); }
    }
  }
}

template <int HF>
DI void p1_half(P1C& c, int& slot, unsigned flip) {
  unsigned delta = 0;
#pragma unroll
  for (int gp = 0; gp < 16; ++gp) {
    const int m = gp >> 1, f = (gp & 1) * 2;
    c.acc[m][HF + f] = mfma(cat(c.Wl[0][f], c.Wh[0][f]), c.X[m], c.acc[m][HF + f]); SCHED();
    if (gp == 0) { asm volatile("" : "+s"(slot)); delta = slot == 2 ? (unsigned)(-2 * (int)ST1) : ST1; asm volatile("" : "+s"(delta)); }
    if (gp >= 2 && !(gp & 1)) c.X[m - 1] = lds_b128(c.ax1 + (m - 1) * 2048);
    if (gp == 1) c.Wl[1][0] = lds_tr(c.aw0 + 8192);
    if (gp == 3) c.Wh[1][0] = lds_tr(c.aw0 + 8192 + 1024);
    if (gp == 5) c.Wl[1][2] = lds_tr(c.aw0 + 16384 + 8192);
    if (gp == 7) c.Wh[1][2] = lds_tr(c.aw0 + 16384 + 8192 + 1024);
    if (gp == 9) { c.aw0 = (c.aw0 + delta) ^ flip; asm volatile("" : "+v"(c.aw0)); }
    if (gp == 11) { c.ax0 += delta; asm volatile("" : "+v"(c.ax0)); }
    if (gp == 13) { slot = slot == 2 ? 0 : slot + 1; asm volatile("" : "+s"(slot)); }
    if (gp == 15) c.X[7] = lds_b128(c.ax1 + 7 * 2048);
    SCHED();
  }
#pragma unroll
  for (int gp = 0; gp < 16; ++gp) {
    const int m = gp >> 1, f = (gp & 1) * 2;
    if (gp == 6) { VIS_LGKM0(); BAR(); SCHED(); }
    c.acc[m][HF + f] = mfma(cat(c.Wl[1][f], c.Wh[1][f]), c.X[m], c.acc[m][HF + f]); SCHED();
    if (gp == 0) { c.ax1 += delta; asm volatile("" : "+v"(c.ax1)); }
    if (gp == 6) { c.Wl[0][0] = lds_tr(c.aw0); c.Wh[0][0] = lds_tr(c.aw0 + 1024); }
    if (gp == 7) c.X[0] = lds_b128(c.ax0);
    if (gp == 8) c.Wl[0][2] = lds_tr(c.aw0 + 16384);
    if (gp == 9) c.Wh[0][2] = lds_tr(c.aw0 + 16384 + 1024);
    if (gp >= 10) c.X[gp - 9] = lds_b128(c.ax0 + (gp - 9) * 2048);
    if (gp == 15) c.X[7] = lds_b128(c.ax0 + 7 * 2048);
    SCHED();
  }
}

template <int HF, bool DIRECT>
DI void p1_epi(P1C& c, unsigned la0, unsigned la2, __amdgpu_buffer_rsrc_t rh, unsigned vo0) {
  u32x4 o[4];
  f32x4 ta = c.acc[0][HF] * -1.44269504088896f, tb = c.acc[1][HF] * -1.44269504088896f;
  SCHED();
#pragma unroll
  for (int m = 0; m < 8; m += 2) {
#pragma unroll
    for (int i = 0; i < 4; ++i) ta[i] = __builtin_amdgcn_exp2f(ta[i]);
#pragma unroll
    for (int i = 0; i < 4; ++i) tb[i] = __builtin_amdgcn_exp2f(tb[i]);
    SCHED();
    f32x4 da = ta + 1.0f, db = tb + 1.0f;
    SCHED();
#pragma unroll
    for (int i = 0; i < 4; ++i) da[i] = __builtin_amdgcn_rcpf(da[i]);
#pragma unroll
    for (int i = 0; i < 4; ++i) db[i] = __builtin_amdgcn_rcpf(db[i]);
    SCHED();
    if (m < 6) { ta = c.acc[m + 2][HF] * -1.44269504088896f; tb = c.acc[m + 3][HF] * -1.44269504088896f; }
    unsigned vo = vo0 + m * (KS32 * 1024);
    if (DIRECT) asm volatile("" : "+v"(vo));
    SCHED();
    da *= c.acc[m][HF]; db *= c.acc[m + 1][HF];
    SCHED();
    da *= c.acc[m][HF + 2]; db *= c.acc[m + 1][HF + 2];
    SCHED();
    const auto s0 = __builtin_amdgcn_permlane16_swap(pk_bf16(da[0], da[1]), pk_bf16(db[0], db[1]), false, false);
    const auto s1 = __builtin_amdgcn_permlane16_swap(pk_bf16(da[2], da[3]), pk_bf16(db[2], db[3]), false, false);
    o[m >> 1] = (u32x4){s0[0], s1[0], s0[1], s1[1]};
    SCHED();
    if (DIRECT) __builtin_amdgcn_raw_buffer_store_b128(o[m >> 1], rh, vo, 0, SC1);
    *(LAS u32x4*)(size_t)(((m & 2) ? la2 : la0) + (m >> 2) * 8192) = o[m >> 1];
    SCHED();
  }
#pragma unroll
  for (int m = 0; m < 4; ++m) asm volatile("" ::"v"(o[m]));
}

struct P2C { bf16x8 H[2][4]; bf16x4 Wl[2][4], Wh[2][4]; f32x4 acc[4][4]; unsigned ah, aw[4]; };

template <int B>
DI void p2_read(P2C& c, int e, unsigned ks) {
  if (e == 2 || e >= 9) { const int m = e == 2 ? 0 : e - 8; c.H[B][m] = lds_b128(c.ah + m * 2048 + ks * 1024); }
  else {
    const int f = e < 2 ? e : e - 1, n = f >> 1;
    if (f & 1) c.Wh[B][n] = lds_tr(c.aw[n] + ks * 8192 + 1024); else c.Wl[B][n] = lds_tr(c.aw[n] + ks * 8192);
  }
}
template <bool FIRST>
DI void p2_tile(P2C& c, int& sw6, int& sh4) {
  unsigned dw = 0, dh = 0;
#pragma unroll
  for (int gp = 0; gp < 16; ++gp) {
    const int m = gp >> 2, n = gp & 3;
    c.acc[m][n] = mfma(cat(c.Wl[0][n], c.Wh[0][n]), c.H[0][m], FIRST ? (f32x4){0.f, 0.f, 0.f, 0.f} : c.acc[m][n]); SCHED();
    if (gp < 12) { p2_read<1>(c, gp, 1); SCHED(); }
    if (gp == 12) { asm volatile("" : "+s"(sw6)); dw = sw6 == 5 ? (unsigned)(-5 * (int)SL2) : SL2; asm volatile("" : "+s"(dw)); SCHED(); }
    if (gp == 13) { asm volatile("" : "+s"(sh4)); dh = sh4 == 3 ? (unsigned)(-3 * (int)SL2) : SL2; asm volatile("" : "+s"(dh)); SCHED(); }
    if (gp == 14) { c.aw[0] += dw; c.aw[1] += dw; asm volatile("" : "+v"(c.aw[0]), "+v"(c.aw[1])); SCHED(); }
    if (gp == 15) { c.aw[2] += dw; c.aw[3] += dw; asm volatile("" : "+v"(c.aw[2]), "+v"(c.aw[3])); SCHED(); }
  }
#pragma unroll
  for (int gp = 0; gp < 16; ++gp) {
    const int m = gp >> 2, n = gp & 3;
    if (gp == 4) { VIS_LGKM0(); BAR(); SCHED(); }
    c.acc[m][n] = mfma(cat(c.Wl[1][n], c.Wh[1][n]), c.H[1][m], c.acc[m][n]); SCHED();
    if (gp == 0) { c.ah += dh; asm volatile("" : "+v"(c.ah)); SCHED(); }
    if (gp == 1) { sw6 = sw6 == 5 ? 0 : sw6 + 1; asm volatile("" : "+s"(sw6)); SCHED(); }
    if (gp == 2) { sh4 = (sh4 + 1) & 3; asm volatile("" : "+s"(sh4)); SCHED(); }
    if (gp >= 4) { p2_read<0>(c, gp - 4, 0); SCHED(); }
  }
}

template <int B>
DI void p2_fread(P2C& c, int e, unsigned ks) {
  if (e == 2 || e >= 9) { const int m = e == 2 ? 1 : e - 7; c.H[B][m] = lds_b128(c.ah + m * 2048 + ks * 1024); }
  else p2_read<B>(c, e, ks);
}
DI void p2_ftile(P2C& c, int& sw6, int& sh4) {
  unsigned dw = 0, dh = 0;
#pragma unroll
  for (int gp = 0; gp < 12; ++gp) {
    const int m = 1 + (gp >> 2), n = gp & 3;
    c.acc[m][n] = mfma(cat(c.Wl[0][n], c.Wh[0][n]), c.H[0][m], c.acc[m][n]); SCHED();
    if (gp < 11) p2_fread<1>(c, gp, 1);
    if (gp == 5) { asm volatile("" : "+s"(sw6)); dw = sw6 == 5 ? (unsigned)(-5 * (int)SL2) : SL2; asm volatile("" : "+s"(dw)); }
    if (gp == 6) { asm volatile("" : "+s"(sh4)); dh = sh4 == 3 ? (unsigned)(-3 * (int)SL2) : SL2; asm volatile("" : "+s"(dh)); }
    if (gp == 7) { c.aw[0] += dw; asm volatile("" : "+v"(c.aw[0])); }
    if (gp == 9) { c.aw[1] += dw; asm volatile("" : "+v"(c.aw[1])); }
    if (gp == 10) { c.aw[2] += dw; asm volatile("" : "+v"(c.aw[2])); }
    if (gp == 11) { c.aw[3] += dw; c.ah += dh; asm volatile("" : "+v"(c.aw[3]), "+v"(c.ah)); }
    SCHED();
  }
  BAR();
  SCHED();
#pragma unroll
  for (int gp = 0; gp < 12; ++gp) {
    const int m = 1 + (gp >> 2), n = gp & 3;
    c.acc[m][n] = mfma(cat(c.Wl[1][n], c.Wh[1][n]), c.H[1][m], c.acc[m][n]); SCHED();
    if (gp < 11) p2_fread<0>(c, gp, 0);
    if (gp == 11) { sw6 = sw6 == 5 ? 0 : sw6 + 1; sh4 = (sh4 + 1) & 3; asm volatile("" : "+s"(sw6), "+s"(sh4)); }
    SCHED();
  }
}
DI void p2_otile(P2C& c, unsigned dw, unsigned dh) {
#pragma unroll
  for (int e = 0; e < 9; ++e) p2_read<0>(c, e, 0);
#pragma unroll
  for (int e = 0; e < 9; ++e) p2_read<1>(c, e, 1);
#pragma unroll
  for (int k = 0; k < 2; ++k)
#pragma unroll
    for (int n = 0; n < 4; ++n) c.acc[0][n] = mfma(cat(c.Wl[k][n], c.Wh[k][n]), c.H[k][0], c.acc[0][n]);
  c.ah += dh;
#pragma unroll
  for (int n = 0; n < 4; ++n) c.aw[n] += dw;
}

__global__ __launch_bounds__(512, 2) void ffn_fused(const __hip_bfloat16*, const __hip_bfloat16*, const __hip_bfloat16*,
                                                    const __hip_bfloat16*, char*, __hip_bfloat16*) {
  extern __shared__ __attribute__((aligned(16))) char smem[];
  u64x4 ka; u64x2 kb;
  asm volatile("s_load_dwordx8 %0, %2, 0x0\n\ts_load_dwordx4 %1, %2, 0x20" : "=&s"(ka), "=&s"(kb) : "s"(__builtin_amdgcn_kernarg_segment_ptr()));
  u64 pc0; asm volatile("s_getpc_b64 %0" : "=s"(pc0));
  const int bid = blockIdx.x, xcd = bid & 7, idx = bid >> 3;
  const int ksp = xcd >> 1, xe = xcd & 1, tb = idx >> 2, u = idx & 3;
  const bool spare = u == 3 && (tb >> 2) != xe;
  const int j = u < 3 ? xe * 4 + u : 3;
  const int fb = ksp * 7 + j, cb = xe * 4 + u;
  const int wave = __builtin_amdgcn_readfirstlane(threadIdx.x >> 6), l = threadIdx.x & 63;
  const unsigned L0 = (unsigned)(size_t)(LAS char*)smem;
  if (wave == 3) touch(mk_rsrc((const void*)(pc0 & ~(u64)127), CODE_BYTES), L0 + 8192 + 768, (idx + (l >> 4) * 32) * 128, 0);
  unsigned vx0, vw0, vp0;
  {
    const int xr = 8 * wave + (l >> 3);
    const unsigned sx = ((xr >> 1) & 3) | ((((xr >> 2) ^ (xr >> 3)) & 1) << 2);
    vx0 = xr * 2048 + (((l & 7) ^ sx) << 4);
    const unsigned sw = (l >> 4) | ((wave & 2) << 1);
    vw0 = (4 * wave + (l >> 4)) * (FF * 2) + (((((l & 15) >> 1) ^ sw)) << 5) + ((l & 1) << 4);
    vp0 = wave ? (idx * 32 + (l >> 1)) * 2048 + (l & 1) * 128
               : ((idx & 15) * 8 + (l >> 3)) * (FF * 2) + (ksp * 7 + xe * 3) * 256 + (l & 7) * 128;
  }
  asm volatile("s_waitcnt lgkmcnt(0)" : "+s"(ka), "+s"(kb), "+v"(vx0), "+v"(vw0), "+v"(vp0));
  const void *x = (const void*)ka.x, *wg = (const void*)ka.y, *wu = (const void*)ka.z, *wd = (const void*)ka.w;
  char* ws = (char*)kb.x;
  void* out = (void*)kb.y;
  GAS u64* hflag = (GAS u64*)(ws + WS_FLAG) + (tb * 4 + ksp) * 128;
  auto unit_table = [&]() {
    const u64 a = (0xCA86420CA86420ull >> (4 * j)) & 0xFFFFFFFull, b = a | 0x1111111ull;
    return spare ? (a | (b << 28)) : ((a & 15ull) | ((b & 15ull) << 4) | ((a >> 4) << 8) | ((b >> 4) << 32));
  };
  const __amdgpu_buffer_rsrc_t rx = mk_rsrc(x, MT * DM * 2), rg = mk_rsrc(wg, DM * FF * 2), ru = mk_rsrc(wu, DM * FF * 2);
  if (wave < 2) touch(wave ? rx : ((idx & 16) ? ru : rg), L0 + 8192 + wave * 256, vp0, 0);
  if (__builtin_expect(!spare, 1)) {
    const unsigned sx0 = tb * 128 * 2048, sw0 = fb * 256, lb = L0 + P1B + wave * 1024;
#pragma unroll
    for (int a = 0; a < 2; ++a) {
      dma16<0>(rg, lb + 16384 + a * 8192, vw0, sw0 + a * 32 * (FF * 2));
      dma16<0>(ru, lb + 32768 + a * 8192, vw0, sw0 + a * 32 * (FF * 2));
      dma16<0>(rx, lb + a * 8192, vx0, sx0 + a * 64 * 2048);
    }
    if (wave >= 4) {
#pragma unroll
      for (int a = 0; a < 2; ++a) {
        dma16<0>(rg, lb + ST1 + 16384 + (a - 1) * 4096, vw0, sw0 + (64 + (a - 1) * 16) * (FF * 2));
        dma16<0>(ru, lb + ST1 + 32768 + (a - 1) * 4096, vw0, sw0 + (64 + (a - 1) * 16) * (FF * 2));
        dma16<0>(rx, lb + ST1 + (a - 1) * 4096, vx0 - 32 * 2048, sx0 + a * 32 * 2048 + 128);
      }
    }
  } else {
    const u64 t_start = __builtin_amdgcn_s_memrealtime();
    const int part = (tb & 3) * 4 + (wave & 3);
    const __amdgpu_buffer_rsrc_t rs = (part & 8) ? ru : rg;
    const unsigned vo = ((part & 7) * 8 + (l >> 3)) * (FF * 2) + (ksp * 7 + xe * 3) * 256 + (l & 7) * 128;
#pragma unroll 1
    for (int t = 2 + (wave >> 2), it = 2; t < 16; t += 2, ++it) {
      touch(rs, L0 + wave * 256, vo, t * 64 * (FF * 2));
      if (it == wave || (it == 2 && wave == 1))
        touch(rx, L0 + wave * 256, (xcd * 128 + (tb & 3) * 32 + (l & 31)) * 2048 + (wave * 2 + (l >> 5)) * 128, 0);
      WAIT_VM(1);
    }
    if (wave < 4 || wave >= 6) {
      const __amdgpu_buffer_rsrc_t rw = mk_rsrc(wd, FF * DM * 2);
      const unsigned vw = (ksp * 896 + (l >> 3)) * 2048 + xe * 1024 + (l & 7) * 128;
#pragma unroll 1
      for (int e = (tb & 3) * 6 + (wave < 4 ? wave : wave - 2); e < 112; e += 24) {
        if (__builtin_amdgcn_s_memrealtime() - t_start > 1000) break;
        const int un = e >> 3, r = (un < 7 ? 2 * un : 2 * un - 13) * 8 + (e & 7);
        touch(rw, L0 + wave * 256, vw, r * 8 * 2048);
        WAIT_VM(1);
      }
    }
    VIS_VM0();
    CFENCE();
  }

  if (wave >= 6) {
    const int lw = wave - 6;
    if (!spare) { WAIT_VM(6); BAR(); }
    u64 tag = __hip_atomic_load((GAS u64*)(ws + WS_EPOCH), RLX_AGENT) + 1ull;
    asm volatile("" : "+v"(tag));
    WAIT_VM(0);
    unsigned w0 = 0, w1 = 0;
    const u64 tbl = unit_table();
    GAS u64* fw;
    { const unsigned un = (unsigned)(tbl >> (4 * ((l >> 2) < 14 ? (l >> 2) : 13))) & 15u; fw = hflag + (un >> 1) * 16 + ((un & 1) ? 4 + (l & 3) : lw); }
    if (!spare) {
#pragma unroll 1
      for (int t = 1; t < 15; ++t) BAR();
      const __amdgpu_buffer_rsrc_t rw = mk_rsrc(wd, FF * DM * 2);
      const int me = idx - ((xe ? (tb < 4 ? tb : 4) : (tb > 4 ? tb - 4 : 0)));
      const unsigned vb = (ksp * 896 + me * 32 + lw * 16 + (l >> 3)) * 2048 + xe * 1024 + (l & 7) * 128;
      w0 = __builtin_amdgcn_raw_buffer_load_b32(rw, vb, 0, 0);
      w1 = __builtin_amdgcn_raw_buffer_load_b32(rw, vb, 8 * 2048, 0);
      BAR();
    }
    BAR();
    const __amdgpu_buffer_rsrc_t rh = mk_rsrc(ws + WS_H, MT * FF * 2);
    unsigned tofs[4];
#pragma unroll
    for (int a = 0; a < 4; ++a) tofs[a] = (unsigned)((tb * 8 + lw * 4 + ((a + ksp) & 3)) * KS32 + ksp * 28) * 1024u;
    auto issue_h = [&](int kt) {
      const unsigned sb = ((unsigned)(tbl >> (4 * kt)) & 15u) * 2048u;
      const unsigned lb = L0 + (kt & 3) * SL2 + lw * 8192;
#pragma unroll
      for (int a = 0; a < 4; ++a) {
        dma16<SC1>(rh, lb + a * 2048, l * 16, sb + tofs[a]);
        dma16<SC1, 1024>(rh, lb + a * 2048, l * 16, sb + tofs[a]);
      }
    };
    int nrdy = 0;
    auto look = [&](u64 v) { nrdy = __builtin_ctzll(~(__ballot(v == tag) | (spare ? 0ull : 255ull)) | (1ull << 63)) >> 2; CFENCE(); };
    if (spare) {
      if (l == 0) __hip_atomic_store(hflag + 7 * 16 + lw, tag, RLX_AGENT);
#pragma unroll 1
      for (unsigned spins = 0; spins < SPIN_CAP && nrdy < 1; ++spins) { __builtin_amdgcn_s_sleep(8); look(__hip_atomic_load(fw, RLX_AGENT)); }
    } else {
      BAR();
      {
        u32x4 v0, v1, v2, v3, v4, v5, v6, v7;
        asm volatile("ds_read_b128 %0, %8\n\tds_read_b128 %1, %8 offset:1024\n\tds_read_b128 %2, %8 offset:2048\n\tds_read_b128 %3, %8 offset:3072\n\t"
                     "ds_read_b128 %4, %8 offset:4096\n\tds_read_b128 %5, %8 offset:5120\n\tds_read_b128 %6, %8 offset:6144\n\tds_read_b128 %7, %8 offset:7168\n\t"
                     "s_waitcnt lgkmcnt(0)"
                     : "=&v"(v0), "=&v"(v1), "=&v"(v2), "=&v"(v3), "=&v"(v4), "=&v"(v5), "=&v"(v6), "=&v"(v7)
                     : "v"(L0 + lw * 8192 + l * 16) : "memory");
        const unsigned sb = (unsigned)j * 4096u;
        st16(v0, rh, l * 16, sb + tofs[0]); st16(v1, rh, l * 16 + 1024, sb + tofs[0]);
        st16(v2, rh, l * 16, sb + tofs[1]); st16(v3, rh, l * 16 + 1024, sb + tofs[1]);
        st16(v4, rh, l * 16, sb + tofs[2]); st16(v5, rh, l * 16 + 1024, sb + tofs[2]);
        st16(v6, rh, l * 16, sb + tofs[3]); st16(v7, rh, l * 16 + 1024, sb + tofs[3]);
      }
      BAR();
      BAR();
      WAIT_VM(0);
      if (l == 0) __hip_atomic_store(hflag + j * 16 + lw, tag, RLX_AGENT);
      BAR();
#pragma unroll 1
      for (int i = 0; i < 3 && nrdy < 4; ++i) look(__hip_atomic_load(fw, RLX_AGENT));
    }
    VIS_VM0();
    asm volatile("" ::"v"(w0), "v"(w1));
    int nx = spare ? 0 : 2;
#pragma unroll 1
    for (int kt = spare ? 0 : 1; kt < 14; ++kt) {
      if (nx == kt + 2) {
        const int fe = nrdy - 3;
#pragma unroll 1
        for (; kt < fe; ++kt) { issue_h(kt + 2); WAIT_VM(16); BAR(); }
        nx = kt + 2;
      }
      const int lim = kt < 11 ? (kt ? kt + 2 : 3) : 13;
      if (__builtin_expect(kt >= nrdy, 0)) {
#pragma unroll 1
        for (unsigned spins = 0; spins < SPIN_CAP && kt >= nrdy; ++spins) look(__hip_atomic_load(fw, RLX_AGENT));
      }
      u64 pv;
      const bool pend = lim + 1 >= nrdy;
      if (__builtin_expect(pend, 0)) asm volatile("global_load_dwordx2 %0, %1, off sc1" : "=v"(pv) : "v"(fw) : "memory");
      const int n0 = nx;
#pragma unroll 1
      while (nx <= lim && nx < nrdy) { issue_h(nx); ++nx; }
      if (__builtin_expect(nx >= kt + 3, 1)) WAIT_VM(16); else if (nx == kt + 2) WAIT_VM(8); else WAIT_VM(0);
      BAR();
      if (__builtin_expect(pend, 0)) {
        if (nx > n0) WAIT_VM(8); else WAIT_VM(0);
        asm volatile("" : "+v"(pv));
        look(pv);
      }
    }
    BAR();
    if (bid == 0 && lw == 0) {
      GAS u64* fa = (GAS u64*)(ws + WS_FLAG);
#pragma unroll 1
      for (unsigned spins = 0; spins < SPIN_CAP; ++spins) {
        bool ok = true;
#pragma unroll
        for (int i = 0; i < 4; ++i) ok &= __hip_atomic_load(fa + (i * 64 + l) * 16, RLX_AGENT) == tag;
        if (__all(ok)) break;
        __builtin_amdgcn_s_sleep(2);
      }
      if (l == 0) __hip_atomic_store((GAS u64*)(ws + WS_EPOCH), tag, RLX_AGENT);
    }
    return;
  }
  if (wave >= 4) {
    const int lw = wave - 4;
    const __amdgpu_buffer_rsrc_t rw = mk_rsrc(wd, FF * DM * 2);
    const unsigned vd = (8 * lw + (l >> 4)) * (DM * 2) + (((((l & 15) >> 1) ^ ((l >> 4) | (lw << 2)))) << 5) + ((l & 1) << 4);
    const unsigned sd0 = cb * 256 + ksp * 896 * (DM * 2);
    const unsigned vd2 = vd + 4 * (DM * 2) - 1024;
    u64 tbl = 0;
    auto issue_w = [&](int kt, unsigned slot) {
      const unsigned sb = sd0 + ((unsigned)(tbl >> (4 * kt)) & 15u) * (64 * DM * 2);
      const unsigned lb = L0 + WB2 + slot * SL2 + lw * 2048;
#pragma unroll
      for (int a = 0; a < 4; ++a) {
        dma16<0>(rw, lb + a * 4096, vd, sb + a * 16 * (DM * 2));
        dma16<0, 1024>(rw, lb + a * 4096, vd2, sb + a * 16 * (DM * 2));
      }
    };
    if (!spare) {
      const int xr = 8 * lw + (l >> 3);
      const unsigned sx = ((xr >> 1) & 3) | ((((xr >> 2) ^ (xr >> 3)) & 1) << 2);
      const unsigned vx = xr * 2048 + (((l & 7) ^ sx) << 4);
      const unsigned sw = (l >> 4) | (lw << 2);
      const unsigned vw = (8 * lw + (l >> 4)) * (FF * 2) + (((((l & 15) >> 1) ^ sw)) << 5) + ((l & 1) << 4);
      const unsigned vw2 = vw + 4 * (FF * 2) - 1024, vx2 = vx + 32768 - 2048;
      unsigned lb = L0 + P1B + ST1 + 8192, so = fb * 256 + 96 * (FF * 2), sxo = tb * 128 * 2048 + 128 + 131072;
      int n = 3;
#pragma unroll 1
      for (int t = 0; t < 14; ++t) {
        if (t == 0) WAIT_VM(6); else if (t == 1) WAIT_VM(12); else WAIT_VM(24);
        BAR();
        const int lim = t ? 2 * t + 6 : 5;
#pragma unroll 1
        for (; n < lim; ++n) {
#pragma unroll
          for (int a = 0; a < 2; ++a) {
            dma16<0>(rg, lb + 16384 + a * 4096 + lw * 2048, vw, so + a * 16 * (FF * 2));
            dma16<0, 1024>(rg, lb + 16384 + a * 4096 + lw * 2048, vw2, so + a * 16 * (FF * 2));
            dma16<0>(ru, lb + 32768 + a * 4096 + lw * 2048, vw, so + a * 16 * (FF * 2));
            dma16<0, 1024>(ru, lb + 32768 + a * 4096 + lw * 2048, vw2, so + a * 16 * (FF * 2));
            dma16<0>(rx, lb + a * 4096 + lw * 1024, vx, sxo + a * 65536);
            dma16<0, 2048>(rx, lb + a * 4096 + lw * 1024, vx2, sxo + a * 65536);
          }
          so += 32 * (FF * 2);
          if (n & 1) { lb += ST1 - 8192; if (lb == L0 + P1B + 3 * ST1) lb = L0 + P1B; sxo += 128 - 131072; }
          else { lb += 8192; sxo += 131072; }
        }
      }
      WAIT_VM(24); BAR();
      WAIT_VM(0); BAR();
      tbl = unit_table();
      BAR();
      BAR();
      BAR();
      issue_w(0, 0);
      BAR();
      issue_w(1, 1);
      WAIT_VM(8); BAR();
#pragma unroll 1
      for (int kt = 2; kt < 4; ++kt) issue_w(kt, kt);
      WAIT_VM(16); BAR();
#pragma unroll 1
      for (int kt = 4; kt < 6; ++kt) issue_w(kt, kt);
      WAIT_VM(24); BAR();
      issue_w(6, 0);
    } else {
      tbl = unit_table();
#pragma unroll 1
      for (int kt = 0; kt < 6; ++kt) issue_w(kt, kt);
      BAR();
      WAIT_VM(0); BAR();
      BAR();
      BAR();
      issue_w(6, 0);
    }
    unsigned slot = 1;
#pragma unroll 1
    for (int kt = 3; kt < 10; ++kt) {
      WAIT_VM(24); BAR();
      issue_w(kt + 4, slot);
      slot = slot == 5 ? 0 : slot + 1;
    }
    WAIT_VM(24); BAR();
    WAIT_VM(16); BAR();
    WAIT_VM(8); BAR();
    WAIT_VM(0); BAR();
    BAR();
    return;
  }

  u64 tag = __hip_atomic_load((GAS u64*)(ws + WS_EPOCH), RLX_AGENT);
  P1C c1;
  {
    const int g = l >> 4, q = (l & 15) >> 2, p = l & 3, r15 = l & 15;
    const unsigned b = L0 + P1B;
    const unsigned sx = ((r15 >> 1) & 3) | ((((r15 >> 2) ^ (r15 >> 3)) & 1) << 2);
    c1.ax0 = b + r15 * 128 + ((g ^ sx) << 4);
    c1.ax1 = b + r15 * 128 + (((4 + g) ^ sx) << 4);
    const unsigned sw = q | ((g & 1) << 2);
    c1.aw0 = b + 16384 + (8 * g + q) * 256 + p * 8 + ((wave ^ sw) << 5);
    c1.aw1 = b + 16384 + (8 * g + q) * 256 + p * 8 + (((4 + wave) ^ sw) << 5);
    asm volatile("" : "+v"(c1.ax0), "+v"(c1.ax1), "+v"(c1.aw0), "+v"(c1.aw1));
  }
#pragma unroll
  for (int m = 0; m < 8; ++m)
#pragma unroll
    for (int n = 0; n < 4; ++n) { c1.acc[m][n] = (f32x4){0.f, 0.f, 0.f, 0.f}; asm volatile("" : "+v"(c1.acc[m][n])); }
  SCHED();
  tag += 1ull;
  tag = ((u64)(unsigned)__builtin_amdgcn_readfirstlane((unsigned)(tag >> 32)) << 32) | (unsigned)__builtin_amdgcn_readfirstlane((unsigned)tag);
  asm volatile("" : "+s"(tag));
  __builtin_amdgcn_s_waitcnt(0x0070);
  SCHED();
  P2C c;
  auto p2_first = [&]() {
    int tz = threadIdx.x; asm volatile("" : "+v"(tz));
    const int l = tz & 63, g = l >> 4, q = (l & 15) >> 2, p = l & 3;
    c.ah = L0 + (wave >> 1) * 8192 + l * 16;
    const unsigned sw = q | ((g & 1) << 2);
#pragma unroll
    for (int n = 0; n < 4; ++n) c.aw[n] = L0 + WB2 + (8 * g + q) * 256 + p * 8 + (((4 * (wave & 1) + n) ^ sw) << 5);
#pragma unroll
    for (int e = 0; e < 12; ++e) p2_read<0>(c, e, 0);
    SCHED();
  };
  if (!spare) {
    BAR();
#pragma unroll
    for (int i = 0; i < 4; ++i) {
      const unsigned a = ((i & 1) ? c1.aw1 : c1.aw0) + (i >> 1) * 16384;
      c1.Wl[0][i] = lds_tr(a); c1.Wh[0][i] = lds_tr(a + 1024);
      if (i == 0) c1.X[0] = lds_b128(c1.ax0);
    }
#pragma unroll
    for (int m = 1; m < 8; ++m) c1.X[m] = lds_b128(c1.ax0 + m * 2048);
    SCHED();
    int slot = 0;
#pragma unroll 1
    for (int t = 0; t < 13; ++t) p1_tile(c1, slot);
#pragma unroll 1
    for (int t = 0; t < 3; ++t) p1_half<0>(c1, slot, t == 2 ? 128u : 0u);
    int tz = threadIdx.x; asm volatile("" : "+v"(tz));
    const unsigned g1 = (tz >> 4) & 1, in = ((wave & 1) * 2 + ((tz >> 5) & 1)) * 256 + (tz & 15) * 16 + (wave >> 1) * 1024;
    const unsigned la0 = L0 + ((g1 - ksp) & 3) * 2048 + in, la2 = L0 + ((g1 + 2 - ksp) & 3) * 2048 + in;
    const __amdgpu_buffer_rsrc_t rh = mk_rsrc(ws + WS_H, MT * FF * 2);
    p1_epi<0, false>(c1, la0, la2, rh, 0u);
    VIS_LGKM0();
    BAR();
#pragma unroll 1
    for (int t = 0; t < 3; ++t) p1_half<1>(c1, slot, 0u);
    p2_first();
    p1_epi<1, true>(c1, la0 + SL2, la2 + SL2, rh, (unsigned)((tb * 8 + g1) * KS32 + fb * 4 + 2) * 1024u + in);
  } else {
    BAR();
    BAR();
    p2_first();
  }

  int tz = threadIdx.x; asm volatile("" : "+v"(tz));
  const int ln = tz & 63;
  int sw6 = 0, sh4 = 0;
  p2_tile<true>(c, sw6, sh4);
  if (!spare) {
    WAIT_VM(0);
    if (ln == 0) __hip_atomic_store(hflag + j * 16 + 4 + wave, tag, RLX_AGENT);
  }
#pragma unroll 1
  for (int kt = 1; kt < 10; ++kt) p2_tile<false>(c, sw6, sh4);
#pragma unroll 1
  for (int kt = 10; kt < 14; ++kt) p2_ftile(c, sw6, sh4);
  const int tile = tb * 8 + cb;
  const __amdgpu_buffer_rsrc_t rp = mk_rsrc(ws + WS_PART, 64u * 4 * 3 * 8192);
#pragma unroll
  for (int mm = 1; mm < 4; ++mm) {
    const unsigned so = (unsigned)(((tile * 4 + ((ksp + mm) & 3)) * 3 + (mm - 1)) * 4 + wave) * 2048u;
#pragma unroll
    for (int n = 0; n < 4; n += 2) {
      const f32x4 a = c.acc[mm][n], b = c.acc[mm][n + 1];
      st16((u32x4){pk_bf16(a[0], a[1]), pk_bf16(a[2], a[3]), pk_bf16(b[0], b[1]), pk_bf16(b[2], b[3])}, rp, ln * 16 + n * 512, so);
    }
  }
#pragma unroll
  for (int n = 0; n < 4; ++n) c.aw[n] += 2 * SL2;
#pragma unroll 1
  for (int kt = 10; kt < 14; ++kt) p2_otile(c, kt == 11 ? (unsigned)(-5 * (int)SL2) : SL2, kt == 11 ? (unsigned)(-3 * (int)SL2) : SL2);
  SCHED();
  GAS u64* xf = (GAS u64*)(ws + WS_XFLAG);
  GAS u64* f = xf + ((tile * 4 + ksp) * 4 + wave) * 8 + (ln < 3 ? ln : 0);
  u64 fl = __hip_atomic_load(f, RLX_AGENT);
  WAIT_VM(1);
  __hip_atomic_store(xf + ((tile * 4 + ((ksp + (ln % 3) + 1) & 3)) * 4 + wave) * 8 + (ln % 3), tag, RLX_AGENT);
  SCHED();
  asm volatile("" : "+v"(fl));
  unsigned seen = (unsigned)__ballot(fl == tag);
  asm volatile("" : "+s"(seen));
  u32x4 px[3][2];
  unsigned got = 0;
#pragma unroll 1
  for (unsigned spins = 0; spins < SPIN_CAP; ++spins) {
    const unsigned now = seen & 7u & ~got;
    CFENCE();
#pragma unroll
    for (int s = 0; s < 3; ++s)
      if (now & (1u << s)) {
        const unsigned so = (unsigned)(((tile * 4 + ksp) * 3 + s) * 4 + wave) * 2048u;
#pragma unroll
        for (int n = 0; n < 2; ++n) px[s][n] = __builtin_amdgcn_raw_buffer_load_b128(rp, ln * 16 + n * 1024, so, SC1);
      }
    got |= now;
    if (got == 7u) break;
    seen = (unsigned)__ballot(__hip_atomic_load(f, RLX_AGENT) == tag);
    asm volatile("" : "+s"(seen));
  }
  unsigned pk[4][2];
#pragma unroll
  for (int n = 0; n < 4; ++n) {
    f32x4 v = c.acc[0][n];
#pragma unroll
    for (int s = 0; s < 3; ++s) {
      const unsigned lo = px[s][n >> 1][(n & 1) * 2], hi = px[s][n >> 1][(n & 1) * 2 + 1];
      v += __builtin_bit_cast(f32x4, (u32x4){lo << 16, lo & 0xFFFF0000u, hi << 16, hi & 0xFFFF0000u});
    }
    pk[n][0] = pk_bf16(v[0], v[1]);
    pk[n][1] = pk_bf16(v[2], v[3]);
  }
  const __amdgpu_buffer_rsrc_t ro = mk_rsrc(out, MT * DM * 2);
  const unsigned g = ln >> 4, row = tb * 128 + (wave >> 1) * 64 + ksp * 16 + (ln & 15);
  const unsigned vo = row * (DM * 2) + (cb * 128 + (wave & 1) * 64 + (g & 1) * 16 + (g >> 1) * 8) * 2;
#pragma unroll
  for (int h = 0; h < 2; ++h) {
    const auto s0 = __builtin_amdgcn_permlane16_swap(pk[2 * h][0], pk[2 * h + 1][0], false, false);
    const auto s1 = __builtin_amdgcn_permlane16_swap(pk[2 * h][1], pk[2 * h + 1][1], false, false);
    st16((u32x4){s0[0], s1[0], s0[1], s1[1]}, ro, vo + h * 64, 0);
  }
}

extern "C" void kernel_launch(void* const* d_in, const int* in_sizes, int n_in, void* d_out, int out_size, void* d_ws,
                              size_t ws_size, hipStream_t stream) {
  (void)hipFuncSetAttribute((const void*)ffn_fused, hipFuncAttributeMaxDynamicSharedMemorySize, LDS_BYTES);
  ffn_fused<<<256, 512, LDS_BYTES, stream>>>((const __hip_bfloat16*)d_in[0], (const __hip_bfloat16*)d_in[1],
                                             (const __hip_bfloat16*)d_in[2], (const __hip_bfloat16*)d_in[3], (char*)d_ws,
                                             (__hip_bfloat16*)d_out);
}
```

```cpp
#include <hip/hip_runtime.h>
#include <hip/hip_bf16.h>

typedef __bf16 bf16x8 __attribute__((ext_vector_type(8)));
typedef __bf16 bf16x4 __attribute__((ext_vector_type(4)));
typedef __bf16 bf16x2 __attribute__((ext_vector_type(2)));
typedef float f32x4 __attribute__((ext_vector_type(4)));
typedef float f32x2 __attribute__((ext_vector_type(2)));
typedef unsigned u32x4 __attribute__((ext_vector_type(4)));
typedef unsigned long long u64;
typedef u64 u64x4 __attribute__((ext_vector_type(4)));
typedef u64 u64x2 __attribute__((ext_vector_type(2)));
#define LAS __attribute__((address_space(3)))
#define GAS __attribute__((address_space(1)))
#define DI __device__ __forceinline__
#define SCHED() __builtin_amdgcn_sched_barrier(0)
#define CFENCE() asm volatile("" ::: "memory")
#define BAR() do { CFENCE(); __builtin_amdgcn_s_barrier(); CFENCE(); } while (0)
#define WAIT_VM(n) asm volatile("s_waitcnt vmcnt(%0)" ::"n"(n) : "memory")
#define VIS_LGKM0() __builtin_amdgcn_s_waitcnt(0xC07F)
#define VIS_VM0() __builtin_amdgcn_s_waitcnt(0x0F70)
#define RLX_AGENT __ATOMIC_RELAXED, __HIP_MEMORY_SCOPE_AGENT

constexpr int MT = 1024, DM = 1024, FF = 3584;
constexpr int KS32 = FF / 32;
constexpr unsigned ST1 = 49152;
constexpr unsigned P1B = 16384;
constexpr unsigned SL2 = 16384;
constexpr unsigned WB2 = 4 * SL2;
constexpr unsigned LDS_BYTES = 163840;
constexpr unsigned SPIN_CAP = 1u << 22;
constexpr unsigned CODE_BYTES = 15616;
constexpr int SC1 = 16;
constexpr size_t WS_H = 0;
constexpr size_t WS_PART = (size_t)MT * FF * 2;
constexpr size_t WS_FLAG = WS_PART + (size_t)64 * 4 * 3 * 8192;
constexpr size_t WS_XFLAG = WS_FLAG + 32768;
constexpr size_t WS_EPOCH = WS_XFLAG + 65536;

DI __amdgpu_buffer_rsrc_t mk_rsrc(const void* p, unsigned bytes) {
  return __builtin_amdgcn_make_buffer_rsrc((void*)p, 0, bytes, 0x00020000);
}
template <int AUX, int IMM = 0>
DI void dma16(__amdgpu_buffer_rsrc_t r, unsigned lds_off, unsigned voff, unsigned soff) {
  __builtin_amdgcn_raw_ptr_buffer_load_lds(r, (LAS void*)(size_t)lds_off, 16, voff, soff, IMM, AUX);
}
DI void touch(__amdgpu_buffer_rsrc_t r, unsigned lds_off, unsigned voff, unsigned soff) {
  __builtin_amdgcn_raw_ptr_buffer_load_lds(r, (LAS void*)(size_t)lds_off, 4, voff, soff, 0, 0);
}
DI void st16(u32x4 v, __amdgpu_buffer_rsrc_t r, unsigned voff, unsigned soff) {
  SCHED();
  __builtin_amdgcn_raw_buffer_store_b128(v, r, voff, soff, SC1);
  asm volatile("s_nop 1");
  SCHED();
}
DI bf16x8 lds_b128(unsigned addr) { return *(LAS bf16x8*)(size_t)addr; }
DI bf16x4 lds_tr(unsigned a) { return __builtin_amdgcn_ds_read_tr16_b64_v4bf16((LAS bf16x4*)(size_t)a); }
DI bf16x8 cat(bf16x4 lo, bf16x4 hi) { return __builtin_shufflevector(lo, hi, 0, 1, 2, 3, 4, 5, 6, 7); }
DI f32x4 mfma(bf16x8 a, bf16x8 b, f32x4 c) { return __builtin_amdgcn_mfma_f32_16x16x32_bf16(a, b, c, 0, 0, 0); }
DI unsigned pk_bf16(float lo, float hi) { return __builtin_bit_cast(unsigned, __builtin_convertvector((f32x2){lo, hi}, bf16x2)); }

struct P1C { bf16x8 X[8]; bf16x4 Wl[2][4], Wh[2][4]; f32x4 acc[8][4]; unsigned ax0, ax1, aw0, aw1; };

DI void p1_tile(P1C& c, int& slot) {
  unsigned delta = 0;
#pragma unroll
  for (int k = 0; k < 2; ++k) {
#pragma unroll
    for (int m = 0; m < 8; ++m) {
      if (k == 1 && m == 2) { VIS_LGKM0(); BAR(); SCHED(); }
      const unsigned ax = k ? c.ax0 : c.ax1;
      const int i = m - 2;
      const unsigned aw = ((i & 1) ? c.aw1 : c.aw0) + (i >> 1) * 16384 + (k ? 0 : 8192);
      c.acc[m][0] = mfma(cat(c.Wl[k][0], c.Wh[k][0]), c.X[m], c.acc[m][0]); SCHED();
      if (m >= 1 + k) { c.X[m - 1 - k] = lds_b128(ax + (m - 1 - k) * 2048); SCHED(); }
      if (k == 1 && m == 0) { c.ax1 += delta; asm volatile("" : "+v"(c.ax1)); SCHED(); }
      c.acc[m][1] = mfma(cat(c.Wl[k][1], c.Wh[k][1]), c.X[m], c.acc[m][1]); SCHED();
      if (m >= 2 && m < 6) { c.Wl[1 - k][i & 3] = lds_tr(aw); SCHED(); }
      if (k == 0 && m == 1) {
        asm volatile("" : "+s"(slot));
        delta = slot == 2 ? (unsigned)(-2 * (int)ST1) : ST1;
        asm volatile("" : "+s"(delta));
        SCHED();
      }
      if (k == 0 && m == 6) { c.ax0 += delta; asm volatile("" : "+v"(c.ax0)); SCHED(); }
      if (k == 0 && m == 7) { c.aw1 += delta; asm volatile("" : "+v"(c.aw1)); SCHED(); }
      c.acc[m][2] = mfma(cat(c.Wl[k][2], c.Wh[k][2]), c.X[m], c.acc[m][2]); SCHED();
      if (m >= 2 && m < 6) { c.Wh[1 - k][i & 3] = lds_tr(aw + 1024); SCHED(); }
      if (m == 7 && k == 1) { c.X[6] = lds_b128(ax + 6 * 2048); SCHED(); }
      if (k == 0 && m == 1) { slot = slot == 2 ? 0 : slot + 1; asm volatile("" : "+s"(slot)); SCHED(); }
      if (k == 0 && m == 6) { c.aw0 += delta; asm volatile("" : "+v"(c.aw0)); SCHED(); }
      c.acc[m][3] = mfma(cat(c.Wl[k][3], c.Wh[k][3]), c.X[m], c.acc[m][3]); SCHED();
      if (m == 7) { c.X[7] = lds_b128(ax + 7 * 2048); SCHED(); }
    }
  }
}

struct P2C { bf16x8 H[2][4]; bf16x4 Wl[2][4], Wh[2][4]; f32x4 acc[4][4]; unsigned ah, aw[4]; };

template <int B>
DI void p2_read(P2C& c, int e, unsigned ks) {
  if (e == 2 || e >= 9) { const int m = e == 2 ? 0 : e - 8; c.H[B][m] = lds_b128(c.ah + m * 2048 + ks * 1024); }
  else {
    const int f = e < 2 ? e : e - 1, n = f >> 1;
    if (f & 1) c.Wh[B][n] = lds_tr(c.aw[n] + ks * 8192 + 1024); else c.Wl[B][n] = lds_tr(c.aw[n] + ks * 8192);
  }
}
template <int HF, bool LAST = false>
DI void p1_half(P1C& c, int& slot, unsigned flip, P2C* c2 = nullptr) {
  unsigned delta = 0;
#pragma unroll
  for (int gp = 0; gp < 16; ++gp) {
    const int m = gp >> 1, f = (gp & 1) * 2;
    c.acc[m][HF + f] = mfma(cat(c.Wl[0][f], c.Wh[0][f]), c.X[m], c.acc[m][HF + f]); SCHED();
    if (gp == 0) { asm volatile("" : "+s"(slot)); delta = slot == 2 ? (unsigned)(-2 * (int)ST1) : ST1; asm volatile("" : "+s"(delta)); }
    if (gp >= 2 && !(gp & 1)) c.X[m - 1] = lds_b128(c.ax1 + (m - 1) * 2048);
    if (gp == 1) c.Wl[1][0] = lds_tr(c.aw0 + 8192);
    if (gp == 3) c.Wh[1][0] = lds_tr(c.aw0 + 8192 + 1024);
    if (gp == 5) c.Wl[1][2] = lds_tr(c.aw0 + 16384 + 8192);
    if (gp == 7) c.Wh[1][2] = lds_tr(c.aw0 + 16384 + 8192 + 1024);
    if (gp == 9) { c.aw0 = (c.aw0 + delta) ^ flip; asm volatile("" : "+v"(c.aw0)); }
    if (gp == 11) { c.ax0 += delta; asm volatile("" : "+v"(c.ax0)); }
    if (gp == 13) { slot = slot == 2 ? 0 : slot + 1; asm volatile("" : "+s"(slot)); }
    if (gp == 15) c.X[7] = lds_b128(c.ax1 + 7 * 2048);
    SCHED();
  }
#pragma unroll
  for (int gp = 0; gp < 16; ++gp) {
    const int m = gp >> 1, f = (gp & 1) * 2;
    if (gp == 6) { VIS_LGKM0(); BAR(); SCHED(); }
    c.acc[m][HF + f] = mfma(cat(c.Wl[1][f], c.Wh[1][f]), c.X[m], c.acc[m][HF + f]); SCHED();
    if (LAST) {
      if (gp >= 6) p2_read<0>(*c2, gp - 6, 0);
      if (gp >= 14) p2_read<0>(*c2, gp - 4, 0);
      SCHED();
      continue;
    }
    if (gp == 0) { c.ax1 += delta; asm volatile("" : "+v"(c.ax1)); }
    if (gp == 6) { c.Wl[0][0] = lds_tr(c.aw0); c.Wh[0][0] = lds_tr(c.aw0 + 1024); }
    if (gp == 7) c.X[0] = lds_b128(c.ax0);
    if (gp == 8) c.Wl[0][2] = lds_tr(c.aw0 + 16384);
    if (gp == 9) c.Wh[0][2] = lds_tr(c.aw0 + 16384 + 1024);
    if (gp >= 10) c.X[gp - 9] = lds_b128(c.ax0 + (gp - 9) * 2048);
    if (gp == 15) c.X[7] = lds_b128(c.ax0 + 7 * 2048);
    SCHED();
  }
}

template <int HF, bool DIRECT>
DI void p1_epi(P1C& c, unsigned la0, unsigned la2, __amdgpu_buffer_rsrc_t rh, unsigned vo0) {
  u32x4 o[4];
  f32x4 ta = c.acc[0][HF] * -1.44269504088896f, tb = c.acc[1][HF] * -1.44269504088896f;
  SCHED();
#pragma unroll
  for (int m = 0; m < 8; m += 2) {
#pragma unroll
    for (int i = 0; i < 4; ++i) ta[i] = __builtin_amdgcn_exp2f(ta[i]);
#pragma unroll
    for (int i = 0; i < 4; ++i) tb[i] = __builtin_amdgcn_exp2f(tb[i]);
    SCHED();
    f32x4 da = ta + 1.0f, db = tb + 1.0f;
    SCHED();
#pragma unroll
    for (int i = 0; i < 4; ++i) da[i] = __builtin_amdgcn_rcpf(da[i]);
#pragma unroll
    for (int i = 0; i < 4; ++i) db[i] = __builtin_amdgcn_rcpf(db[i]);
    SCHED();
    if (m < 6) { ta = c.acc[m + 2][HF] * -1.44269504088896f; tb = c.acc[m + 3][HF] * -1.44269504088896f; }
    unsigned vo = vo0 + m * (KS32 * 1024);
    if (DIRECT) asm volatile("" : "+v"(vo));
    SCHED();
    da *= c.acc[m][HF]; db *= c.acc[m + 1][HF];
    SCHED();
    da *= c.acc[m][HF + 2]; db *= c.acc[m + 1][HF + 2];
    SCHED();
    const auto s0 = __builtin_amdgcn_permlane16_swap(pk_bf16(da[0], da[1]), pk_bf16(db[0], db[1]), false, false);
    const auto s1 = __builtin_amdgcn_permlane16_swap(pk_bf16(da[2], da[3]), pk_bf16(db[2], db[3]), false, false);
    o[m >> 1] = (u32x4){s0[0], s1[0], s0[1], s1[1]};
    SCHED();
    if (DIRECT) __builtin_amdgcn_raw_buffer_store_b128(o[m >> 1], rh, vo, 0, SC1);
    *(LAS u32x4*)(size_t)(((m & 2) ? la2 : la0) + (m >> 2) * 8192) = o[m >> 1];
    SCHED();
  }
#pragma unroll
  for (int m = 0; m < 4; ++m) asm volatile("" ::"v"(o[m]));
}

template <bool FIRST>
DI void p2_tile(P2C& c, int& sw6, int& sh4) {
  unsigned dw = 0, dh = 0;
#pragma unroll
  for (int gp = 0; gp < 16; ++gp) {
    const int m = gp >> 2, n = gp & 3;
    c.acc[m][n] = mfma(cat(c.Wl[0][n], c.Wh[0][n]), c.H[0][m], FIRST ? (f32x4){0.f, 0.f, 0.f, 0.f} : c.acc[m][n]); SCHED();
    if (gp < 12) { p2_read<1>(c, gp, 1); SCHED(); }
    if (gp == 12) { asm volatile("" : "+s"(sw6)); dw = sw6 == 5 ? (unsigned)(-5 * (int)SL2) : SL2; asm volatile("" : "+s"(dw)); SCHED(); }
    if (gp == 13) { asm volatile("" : "+s"(sh4)); dh = sh4 == 3 ? (unsigned)(-3 * (int)SL2) : SL2; asm volatile("" : "+s"(dh)); SCHED(); }
    if (gp == 14) { c.aw[0] += dw; c.aw[1] += dw; asm volatile("" : "+v"(c.aw[0]), "+v"(c.aw[1])); SCHED(); }
    if (gp == 15) { c.aw[2] += dw; c.aw[3] += dw; asm volatile("" : "+v"(c.aw[2]), "+v"(c.aw[3])); SCHED(); }
  }
#pragma unroll
  for (int gp = 0; gp < 16; ++gp) {
    const int m = gp >> 2, n = gp & 3;
    if (gp == 4) { VIS_LGKM0(); BAR(); SCHED(); }
    c.acc[m][n] = mfma(cat(c.Wl[1][n], c.Wh[1][n]), c.H[1][m], c.acc[m][n]); SCHED();
    if (gp == 0) { c.ah += dh; asm volatile("" : "+v"(c.ah)); SCHED(); }
    if (gp == 1) { sw6 = sw6 == 5 ? 0 : sw6 + 1; asm volatile("" : "+s"(sw6)); SCHED(); }
    if (gp == 2) { sh4 = (sh4 + 1) & 3; asm volatile("" : "+s"(sh4)); SCHED(); }
    if (gp >= 4) { p2_read<0>(c, gp - 4, 0); SCHED(); }
  }
}

template <int B>
DI void p2_fread(P2C& c, int e, unsigned ks) {
  if (e == 2 || e >= 9) { const int m = e == 2 ? 1 : e - 7; c.H[B][m] = lds_b128(c.ah + m * 2048 + ks * 1024); }
  else p2_read<B>(c, e, ks);
}
DI void p2_ftile(P2C& c, int& sw6, int& sh4) {
  unsigned dw = 0, dh = 0;
#pragma unroll
  for (int gp = 0; gp < 12; ++gp) {
    const int m = 1 + (gp >> 2), n = gp & 3;
    c.acc[m][n] = mfma(cat(c.Wl[0][n], c.Wh[0][n]), c.H[0][m], c.acc[m][n]); SCHED();
    if (gp < 11) p2_fread<1>(c, gp, 1);
    if (gp == 5) { asm volatile("" : "+s"(sw6)); dw = sw6 == 5 ? (unsigned)(-5 * (int)SL2) : SL2; asm volatile("" : "+s"(dw)); }
    if (gp == 6) { asm volatile("" : "+s"(sh4)); dh = sh4 == 3 ? (unsigned)(-3 * (int)SL2) : SL2; asm volatile("" : "+s"(dh)); }
    if (gp == 7) { c.aw[0] += dw; asm volatile("" : "+v"(c.aw[0])); }
    if (gp == 9) { c.aw[1] += dw; asm volatile("" : "+v"(c.aw[1])); }
    if (gp == 10) { c.aw[2] += dw; asm volatile("" : "+v"(c.aw[2])); }
    if (gp == 11) { c.aw[3] += dw; c.ah += dh; asm volatile("" : "+v"(c.aw[3]), "+v"(c.ah)); }
    SCHED();
  }
  BAR();
  SCHED();
#pragma unroll
  for (int gp = 0; gp < 12; ++gp) {
    const int m = 1 + (gp >> 2), n = gp & 3;
    c.acc[m][n] = mfma(cat(c.Wl[1][n], c.Wh[1][n]), c.H[1][m], c.acc[m][n]); SCHED();
    if (gp < 11) p2_fread<0>(c, gp, 0);
    if (gp == 11) { sw6 = sw6 == 5 ? 0 : sw6 + 1; sh4 = (sh4 + 1) & 3; asm volatile("" : "+s"(sw6), "+s"(sh4)); }
    SCHED();
  }
}
DI void p2_otile(P2C& c, unsigned dw, unsigned dh) {
#pragma unroll
  for (int e = 0; e < 9; ++e) p2_read<0>(c, e, 0);
#pragma unroll
  for (int e = 0; e < 9; ++e) p2_read<1>(c, e, 1);
#pragma unroll
  for (int k = 0; k < 2; ++k)
#pragma unroll
    for (int n = 0; n < 4; ++n) c.acc[0][n] = mfma(cat(c.Wl[k][n], c.Wh[k][n]), c.H[k][0], c.acc[0][n]);
  c.ah += dh;
#pragma unroll
  for (int n = 0; n < 4; ++n) c.aw[n] += dw;
}

__global__ __launch_bounds__(512, 2) void ffn_fused(const __hip_bfloat16*, const __hip_bfloat16*, const __hip_bfloat16*,
                                                    const __hip_bfloat16*, char*, __hip_bfloat16*) {
  extern __shared__ __attribute__((aligned(16))) char smem[];
  u64x4 ka; u64x2 kb;
  asm volatile("s_load_dwordx8 %0, %2, 0x0\n\ts_load_dwordx4 %1, %2, 0x20" : "=&s"(ka), "=&s"(kb) : "s"(__builtin_amdgcn_kernarg_segment_ptr()));
  u64 pc0; asm volatile("s_getpc_b64 %0" : "=s"(pc0));
  const int bid = blockIdx.x, xcd = bid & 7, idx = bid >> 3;
  const int ksp = xcd >> 1, xe = xcd & 1, tb = idx >> 2, u = idx & 3;
  const bool spare = u == 3 && (tb >> 2) != xe;
  const int j = u < 3 ? xe * 4 + u : 3;
  const int fb = ksp * 7 + j, cb = xe * 4 + u;
  const int wave = __builtin_amdgcn_readfirstlane(threadIdx.x >> 6), l = threadIdx.x & 63;
  const unsigned L0 = (unsigned)(size_t)(LAS char*)smem;
  if (wave == 3) touch(mk_rsrc((const void*)(pc0 & ~(u64)127), CODE_BYTES), L0 + 8192 + 768, (idx + (l >> 4) * 32) * 128, 0);
  unsigned vx0, vw0, vp0;
  {
    const int xr = 8 * wave + (l >> 3);
    const unsigned sx = ((xr >> 1) & 3) | ((((xr >> 2) ^ (xr >> 3)) & 1) << 2);
    vx0 = xr * 2048 + (((l & 7) ^ sx) << 4);
    const unsigned sw = (l >> 4) | ((wave & 2) << 1);
    vw0 = (4 * wave + (l >> 4)) * (FF * 2) + (((((l & 15) >> 1) ^ sw)) << 5) + ((l & 1) << 4);
    vp0 = wave ? (idx * 32 + (l >> 1)) * 2048 + (l & 1) * 128
               : ((idx & 15) * 8 + (l >> 3)) * (FF * 2) + (ksp * 7 + xe * 3) * 256 + (l & 7) * 128;
  }
  asm volatile("s_waitcnt lgkmcnt(0)" : "+s"(ka), "+s"(kb), "+v"(vx0), "+v"(vw0), "+v"(vp0));
  const void *x = (const void*)ka.x, *wg = (const void*)ka.y, *wu = (const void*)ka.z, *wd = (const void*)ka.w;
  char* ws = (char*)kb.x;
  void* out = (void*)kb.y;
  u64 tag = __hip_atomic_load((GAS u64*)(ws + WS_EPOCH), RLX_AGENT);
  CFENCE();
  GAS u64* hflag = (GAS u64*)(ws + WS_FLAG) + (tb * 4 + ksp) * 128;
  auto unit_table = [&]() {
    const u64 a = (0xCA86420CA86420ull >> (4 * j)) & 0xFFFFFFFull, b = a | 0x1111111ull;
    return spare ? (a | (b << 28)) : ((a & 15ull) | ((b & 15ull) << 4) | ((a >> 4) << 8) | ((b >> 4) << 32));
  };
  const __amdgpu_buffer_rsrc_t rx = mk_rsrc(x, MT * DM * 2), rg = mk_rsrc(wg, DM * FF * 2), ru = mk_rsrc(wu, DM * FF * 2);
  if (wave < 2) touch(wave ? rx : ((idx & 16) ? ru : rg), L0 + 8192 + wave * 256, vp0, 0);
  if (__builtin_expect(!spare, 1)) {
    const unsigned sx0 = tb * 128 * 2048, sw0 = fb * 256, lb = L0 + P1B + wave * 1024;
#pragma unroll
    for (int a = 0; a < 2; ++a) {
      dma16<0>(rg, lb + 16384 + a * 8192, vw0, sw0 + a * 32 * (FF * 2));
      dma16<0>(ru, lb + 32768 + a * 8192, vw0, sw0 + a * 32 * (FF * 2));
      dma16<0>(rx, lb + a * 8192, vx0, sx0 + a * 64 * 2048);
    }
    if (wave >= 4) {
#pragma unroll
      for (int a = 0; a < 2; ++a) {
        dma16<0>(rg, lb + ST1 + 16384 + (a - 1) * 4096, vw0, sw0 + (64 + (a - 1) * 16) * (FF * 2));
        dma16<0>(ru, lb + ST1 + 32768 + (a - 1) * 4096, vw0, sw0 + (64 + (a - 1) * 16) * (FF * 2));
        dma16<0>(rx, lb + ST1 + (a - 1) * 4096, vx0 - 32 * 2048, sx0 + a * 32 * 2048 + 128);
      }
    }
  } else {
    const u64 t_start = __builtin_amdgcn_s_memrealtime();
    const int part = (tb & 3) * 4 + (wave & 3);
    const __amdgpu_buffer_rsrc_t rs = (part & 8) ? ru : rg;
    const unsigned vo = ((part & 7) * 8 + (l >> 3)) * (FF * 2) + (ksp * 7 + xe * 3) * 256 + (l & 7) * 128;
#pragma unroll 1
    for (int t = 2 + (wave >> 2), it = 2; t < 16; t += 2, ++it) {
      touch(rs, L0 + wave * 256, vo, t * 64 * (FF * 2));
      if (it == wave || (it == 2 && wave == 1))
        touch(rx, L0 + wave * 256, (xcd * 128 + (tb & 3) * 32 + (l & 31)) * 2048 + (wave * 2 + (l >> 5)) * 128, 0);
      WAIT_VM(1);
    }
    if (wave < 4 || wave >= 6) {
      const __amdgpu_buffer_rsrc_t rw = mk_rsrc(wd, FF * DM * 2);
      const unsigned vw = (ksp * 896 + (l >> 3)) * 2048 + xe * 1024 + (l & 7) * 128;
#pragma unroll 1
      for (int e = (tb & 3) * 6 + (wave < 4 ? wave : wave - 2); e < 112; e += 24) {
        if (__builtin_amdgcn_s_memrealtime() - t_start > 1000) break;
        const int un = e >> 3, r = (un < 7 ? 2 * un : 2 * un - 13) * 8 + (e & 7);
        touch(rw, L0 + wave * 256, vw, r * 8 * 2048);
        WAIT_VM(1);
      }
    }
    VIS_VM0();
    CFENCE();
  }

  if (wave >= 6) {
    const int lw = wave - 6;
    if (!spare) { WAIT_VM(6); BAR(); }
    asm volatile("" : "+v"(tag));
    tag += 1ull;
    asm volatile("" : "+v"(tag));
    WAIT_VM(0);
    unsigned w0 = 0, w1 = 0;
    const u64 tbl = unit_table();
    GAS u64* fw;
    { const unsigned un = (unsigned)(tbl >> (4 * ((l >> 2) < 14 ? (l >> 2) : 13))) & 15u; fw = hflag + (un >> 1) * 16 + ((un & 1) ? 4 + (l & 3) : lw); }
    if (!spare) {
#pragma unroll 1
      for (int t = 1; t < 15; ++t) BAR();
      const __amdgpu_buffer_rsrc_t rw = mk_rsrc(wd, FF * DM * 2);
      const int me = idx - ((xe ? (tb < 4 ? tb : 4) : (tb > 4 ? tb - 4 : 0)));
      const unsigned vb = (ksp * 896 + me * 32 + lw * 16 + (l >> 3)) * 2048 + xe * 1024 + (l & 7) * 128;
      w0 = __builtin_amdgcn_raw_buffer_load_b32(rw, vb, 0, 0);
      w1 = __builtin_amdgcn_raw_buffer_load_b32(rw, vb, 8 * 2048, 0);
      BAR();
    }
    BAR();
    const __amdgpu_buffer_rsrc_t rh = mk_rsrc(ws + WS_H, MT * FF * 2);
    unsigned tofs[4];
#pragma unroll
    for (int a = 0; a < 4; ++a) tofs[a] = (unsigned)((tb * 8 + lw * 4 + ((a + ksp) & 3)) * KS32 + ksp * 28) * 1024u;
    auto issue_h = [&](int kt) {
      const unsigned sb = ((unsigned)(tbl >> (4 * kt)) & 15u) * 2048u;
      const unsigned lb = L0 + (kt & 3) * SL2 + lw * 8192;
#pragma unroll
      for (int a = 0; a < 4; ++a) {
        dma16<SC1>(rh, lb + a * 2048, l * 16, sb + tofs[a]);
        dma16<SC1, 1024>(rh, lb + a * 2048, l * 16, sb + tofs[a]);
      }
    };
    int nrdy = 0;
    auto look = [&](u64 v) { nrdy = __builtin_ctzll(~(__ballot(v == tag) | (spare ? 0ull : 255ull)) | (1ull << 63)) >> 2; CFENCE(); };
    if (spare) {
      if (l == 0) __hip_atomic_store(hflag + 7 * 16 + lw, tag, RLX_AGENT);
#pragma unroll 1
      for (unsigned spins = 0; spins < SPIN_CAP && nrdy < 1; ++spins) { __builtin_amdgcn_s_sleep(8); look(__hip_atomic_load(fw, RLX_AGENT)); }
    } else {
      BAR();
      {
        u32x4 v0, v1, v2, v3, v4, v5, v6, v7;
        asm volatile("ds_read_b128 %0, %8\n\tds_read_b128 %1, %8 offset:1024\n\tds_read_b128 %2, %8 offset:2048\n\tds_read_b128 %3, %8 offset:3072\n\t"
                     "ds_read_b128 %4, %8 offset:4096\n\tds_read_b128 %5, %8 offset:5120\n\tds_read_b128 %6, %8 offset:6144\n\tds_read_b128 %7, %8 offset:7168\n\t"
                     "s_waitcnt lgkmcnt(0)"
                     : "=&v"(v0), "=&v"(v1), "=&v"(v2), "=&v"(v3), "=&v"(v4), "=&v"(v5), "=&v"(v6), "=&v"(v7)
                     : "v"(L0 + lw * 8192 + l * 16) : "memory");
        const unsigned sb = (unsigned)j * 4096u;
        st16(v0, rh, l * 16, sb + tofs[0]); st16(v1, rh, l * 16 + 1024, sb + tofs[0]);
        st16(v2, rh, l * 16, sb + tofs[1]); st16(v3, rh, l * 16 + 1024, sb + tofs[1]);
        st16(v4, rh, l * 16, sb + tofs[2]); st16(v5, rh, l * 16 + 1024, sb + tofs[2]);
        st16(v6, rh, l * 16, sb + tofs[3]); st16(v7, rh, l * 16 + 1024, sb + tofs[3]);
      }
      BAR();
      BAR();
      WAIT_VM(0);
      if (l == 0) __hip_atomic_store(hflag + j * 16 + lw, tag, RLX_AGENT);
      BAR();
#pragma unroll 1
      for (int i = 0; i < 3 && nrdy < 4; ++i) look(__hip_atomic_load(fw, RLX_AGENT));
    }
    VIS_VM0();
    asm volatile("" ::"v"(w0), "v"(w1));
    int nx = spare ? 0 : 2;
#pragma unroll 1
    for (int kt = spare ? 0 : 1; kt < 14; ++kt) {
      if (nx == kt + 2) {
        const int fe = nrdy - 3;
#pragma unroll 1
        for (; kt < fe; ++kt) { issue_h(kt + 2); WAIT_VM(16); BAR(); }
        nx = kt + 2;
      }
      const int lim = kt < 11 ? (kt ? kt + 2 : 3) : 13;
      if (__builtin_expect(kt >= nrdy, 0)) {
#pragma unroll 1
        for (unsigned spins = 0; spins < SPIN_CAP && kt >= nrdy; ++spins) look(__hip_atomic_load(fw, RLX_AGENT));
      }
      u64 pv;
      const bool pend = lim + 1 >= nrdy;
      if (__builtin_expect(pend, 0)) asm volatile("global_load_dwordx2 %0, %1, off sc1" : "=v"(pv) : "v"(fw) : "memory");
      const int n0 = nx;
#pragma unroll 1
      while (nx <= lim && nx < nrdy) { issue_h(nx); ++nx; }
      if (__builtin_expect(nx >= kt + 3, 1)) WAIT_VM(16); else if (nx == kt + 2) WAIT_VM(8); else WAIT_VM(0);
      BAR();
      if (__builtin_expect(pend, 0)) {
        if (nx > n0) WAIT_VM(8); else WAIT_VM(0);
        asm volatile("" : "+v"(pv));
        look(pv);
      }
    }
    BAR();
    if (bid == 0 && lw == 0) {
      GAS u64* fa = (GAS u64*)(ws + WS_FLAG);
#pragma unroll 1
      for (unsigned spins = 0; spins < SPIN_CAP; ++spins) {
        bool ok = true;
#pragma unroll
        for (int i = 0; i < 4; ++i) ok &= __hip_atomic_load(fa + (i * 64 + l) * 16, RLX_AGENT) == tag;
        if (__all(ok)) break;
        __builtin_amdgcn_s_sleep(2);
      }
      if (l == 0) __hip_atomic_store((GAS u64*)(ws + WS_EPOCH), tag, RLX_AGENT);
    }
    return;
  }
  if (wave >= 4) {
    const int lw = wave - 4;
    const __amdgpu_buffer_rsrc_t rw = mk_rsrc(wd, FF * DM * 2);
    const unsigned vd = (8 * lw + (l >> 4)) * (DM * 2) + (((((l & 15) >> 1) ^ ((l >> 4) | (lw << 2)))) << 5) + ((l & 1) << 4);
    const unsigned sd0 = cb * 256 + ksp * 896 * (DM * 2);
    const unsigned vd2 = vd + 4 * (DM * 2) - 1024;
    u64 tbl = 0;
    auto issue_w = [&](int kt, unsigned slot) {
      const unsigned sb = sd0 + ((unsigned)(tbl >> (4 * kt)) & 15u) * (64 * DM * 2);
      const unsigned lb = L0 + WB2 + slot * SL2 + lw * 2048;
#pragma unroll
      for (int a = 0; a < 4; ++a) {
        dma16<0>(rw, lb + a * 4096, vd, sb + a * 16 * (DM * 2));
        dma16<0, 1024>(rw, lb + a * 4096, vd2, sb + a * 16 * (DM * 2));
      }
    };
    if (!spare) {
      const int xr = 8 * lw + (l >> 3);
      const unsigned sx = ((xr >> 1) & 3) | ((((xr >> 2) ^ (xr >> 3)) & 1) << 2);
      const unsigned vx = xr * 2048 + (((l & 7) ^ sx) << 4);
      const unsigned sw = (l >> 4) | (lw << 2);
      const unsigned vw = (8 * lw + (l >> 4)) * (FF * 2) + (((((l & 15) >> 1) ^ sw)) << 5) + ((l & 1) << 4);
      const unsigned vw2 = vw + 4 * (FF * 2) - 1024, vx2 = vx + 32768 - 2048;
      unsigned lb = L0 + P1B + ST1 + 8192, so = fb * 256 + 96 * (FF * 2), sxo = tb * 128 * 2048 + 128 + 131072;
      int n = 3;
#pragma unroll 1
      for (int t = 0; t < 14; ++t) {
        if (t == 0) WAIT_VM(6); else if (t == 1) WAIT_VM(12); else WAIT_VM(24);
        BAR();
        const int lim = t ? 2 * t + 6 : 5;
#pragma unroll 1
        for (; n < lim; ++n) {
#pragma unroll
          for (int a = 0; a < 2; ++a) {
            dma16<0>(rg, lb + 16384 + a * 4096 + lw * 2048, vw, so + a * 16 * (FF * 2));
            dma16<0, 1024>(rg, lb + 16384 + a * 4096 + lw * 2048, vw2, so + a * 16 * (FF * 2));
            dma16<0>(ru, lb + 32768 + a * 4096 + lw * 2048, vw, so + a * 16 * (FF * 2));
            dma16<0, 1024>(ru, lb + 32768 + a * 4096 + lw * 2048, vw2, so + a * 16 * (FF * 2));
            dma16<0>(rx, lb + a * 4096 + lw * 1024, vx, sxo + a * 65536);
            dma16<0, 2048>(rx, lb + a * 4096 + lw * 1024, vx2, sxo + a * 65536);
          }
          so += 32 * (FF * 2);
          if (n & 1) { lb += ST1 - 8192; if (lb == L0 + P1B + 3 * ST1) lb = L0 + P1B; sxo += 128 - 131072; }
          else { lb += 8192; sxo += 131072; }
        }
      }
      WAIT_VM(24); BAR();
      WAIT_VM(0); BAR();
      tbl = unit_table();
      BAR();
      BAR();
      BAR();
      issue_w(0, 0);
      BAR();
      issue_w(1, 1);
      WAIT_VM(8); BAR();
#pragma unroll 1
      for (int kt = 2; kt < 4; ++kt) issue_w(kt, kt);
      WAIT_VM(16); BAR();
#pragma unroll 1
      for (int kt = 4; kt < 6; ++kt) issue_w(kt, kt);
      WAIT_VM(24); BAR();
      issue_w(6, 0);
    } else {
      tbl = unit_table();
#pragma unroll 1
      for (int kt = 0; kt < 6; ++kt) issue_w(kt, kt);
      BAR();
      WAIT_VM(0); BAR();
      BAR();
      BAR();
      issue_w(6, 0);
    }
    unsigned slot = 1;
#pragma unroll 1
    for (int kt = 3; kt < 10; ++kt) {
      WAIT_VM(24); BAR();
      issue_w(kt + 4, slot);
      slot = slot == 5 ? 0 : slot + 1;
    }
    WAIT_VM(24); BAR();
    WAIT_VM(16); BAR();
    WAIT_VM(8); BAR();
    WAIT_VM(0); BAR();
    BAR();
    return;
  }

  P1C c1;
  P2C c;
  unsigned la0, la2, vo0;
  {
    const int g = l >> 4, q = (l & 15) >> 2, p = l & 3, r15 = l & 15;
    const unsigned b = L0 + P1B;
    const unsigned sx = ((r15 >> 1) & 3) | ((((r15 >> 2) ^ (r15 >> 3)) & 1) << 2);
    c1.ax0 = b + r15 * 128 + ((g ^ sx) << 4);
    c1.ax1 = b + r15 * 128 + (((4 + g) ^ sx) << 4);
    const unsigned sw = q | ((g & 1) << 2);
    c1.aw0 = b + 16384 + (8 * g + q) * 256 + p * 8 + ((wave ^ sw) << 5);
    c1.aw1 = b + 16384 + (8 * g + q) * 256 + p * 8 + (((4 + wave) ^ sw) << 5);
    asm volatile("" : "+v"(c1.ax0), "+v"(c1.ax1), "+v"(c1.aw0), "+v"(c1.aw1));
    c.ah = L0 + (wave >> 1) * 8192 + l * 16;
#pragma unroll
    for (int n = 0; n < 4; ++n) c.aw[n] = L0 + WB2 + (8 * g + q) * 256 + p * 8 + (((4 * (wave & 1) + n) ^ sw) << 5);
    const unsigned g1 = g & 1, in = ((wave & 1) * 2 + (g >> 1)) * 256 + r15 * 16 + (wave >> 1) * 1024;
    la0 = L0 + ((g1 - ksp) & 3) * 2048 + in; la2 = L0 + ((g1 + 2 - ksp) & 3) * 2048 + in;
    vo0 = (unsigned)((tb * 8 + g1) * KS32 + fb * 4 + 2) * 1024u + in;
    asm volatile("" : "+v"(c.ah), "+v"(c.aw[0]), "+v"(c.aw[1]), "+v"(c.aw[2]), "+v"(c.aw[3]), "+v"(la0), "+v"(la2), "+v"(vo0));
  }
#pragma unroll
  for (int m = 0; m < 8; ++m)
#pragma unroll
    for (int n = 0; n < 4; ++n) { c1.acc[m][n] = (f32x4){0.f, 0.f, 0.f, 0.f}; asm volatile("" : "+v"(c1.acc[m][n])); }
  SCHED();
  asm volatile("" : "+v"(tag));
  tag += 1ull;
  tag = ((u64)(unsigned)__builtin_amdgcn_readfirstlane((unsigned)(tag >> 32)) << 32) | (unsigned)__builtin_amdgcn_readfirstlane((unsigned)tag);
  asm volatile("" : "+s"(tag));
  __builtin_amdgcn_s_waitcnt(0x0070);
  SCHED();
  if (!spare) {
    BAR();
#pragma unroll
    for (int i = 0; i < 4; ++i) {
      const unsigned a = ((i & 1) ? c1.aw1 : c1.aw0) + (i >> 1) * 16384;
      c1.Wl[0][i] = lds_tr(a); c1.Wh[0][i] = lds_tr(a + 1024);
      if (i == 0) c1.X[0] = lds_b128(c1.ax0);
    }
#pragma unroll
    for (int m = 1; m < 8; ++m) c1.X[m] = lds_b128(c1.ax0 + m * 2048);
    SCHED();
    int slot = 0;
#pragma unroll 1
    for (int t = 0; t < 13; ++t) p1_tile(c1, slot);
#pragma unroll 1
    for (int t = 0; t < 3; ++t) p1_half<0>(c1, slot, t == 2 ? 128u : 0u);
    const __amdgpu_buffer_rsrc_t rh = mk_rsrc(ws + WS_H, MT * FF * 2);
    p1_epi<0, false>(c1, la0, la2, rh, 0u);
    VIS_LGKM0();
    BAR();
#pragma unroll 1
    for (int t = 0; t < 2; ++t) p1_half<1>(c1, slot, 0u);
    p1_half<1, true>(c1, slot, 0u, &c);
    p1_epi<1, true>(c1, la0 + SL2, la2 + SL2, rh, vo0);
  } else {
    BAR();
    BAR();
#pragma unroll
    for (int e = 0; e < 12; ++e) p2_read<0>(c, e, 0);
    SCHED();
  }

  int tz = threadIdx.x; asm volatile("" : "+v"(tz));
  const int ln = tz & 63;
  int sw6 = 0, sh4 = 0;
  p2_tile<true>(c, sw6, sh4);
  if (!spare) {
    WAIT_VM(0);
    if (ln == 0) __hip_atomic_store(hflag + j * 16 + 4 + wave, tag, RLX_AGENT);
  }
#pragma unroll 1
  for (int kt = 1; kt < 10; ++kt) p2_tile<false>(c, sw6, sh4);
#pragma unroll 1
  for (int kt = 10; kt < 14; ++kt) p2_ftile(c, sw6, sh4);
  const int tile = tb * 8 + cb;
  const __amdgpu_buffer_rsrc_t rp = mk_rsrc(ws + WS_PART, 64u * 4 * 3 * 8192);
#pragma unroll
  for (int mm = 1; mm < 4; ++mm) {
    const unsigned so = (unsigned)(((tile * 4 + ((ksp + mm) & 3)) * 3 + (mm - 1)) * 4 + wave) * 2048u;
#pragma unroll
    for (int n = 0; n < 4; n += 2) {
      const f32x4 a = c.acc[mm][n], b = c.acc[mm][n + 1];
      st16((u32x4){pk_bf16(a[0], a[1]), pk_bf16(a[2], a[3]), pk_bf16(b[0], b[1]), pk_bf16(b[2], b[3])}, rp, ln * 16 + n * 512, so);
    }
  }
#pragma unroll
  for (int n = 0; n < 4; ++n) c.aw[n] += 2 * SL2;
#pragma unroll 1
  for (int kt = 10; kt < 14; ++kt) p2_otile(c, kt == 11 ? (unsigned)(-5 * (int)SL2) : SL2, kt == 11 ? (unsigned)(-3 * (int)SL2) : SL2);
  SCHED();
  GAS u64* xf = (GAS u64*)(ws + WS_XFLAG);
  GAS u64* f = xf + ((tile * 4 + ksp) * 4 + wave) * 8 + (ln < 3 ? ln : 0);
  u64 fl = __hip_atomic_load(f, RLX_AGENT);
  WAIT_VM(1);
  __hip_atomic_store(xf + ((tile * 4 + ((ksp + (ln % 3) + 1) & 3)) * 4 + wave) * 8 + (ln % 3), tag, RLX_AGENT);
  SCHED();
  asm volatile("" : "+v"(fl));
  unsigned seen = (unsigned)__ballot(fl == tag);
  asm volatile("" : "+s"(seen));
  u32x4 px[3][2];
  unsigned got = 0;
#pragma unroll 1
  for (unsigned spins = 0; spins < SPIN_CAP; ++spins) {
    const unsigned now = seen & 7u & ~got;
    CFENCE();
#pragma unroll
    for (int s = 0; s < 3; ++s)
      if (now & (1u << s)) {
        const unsigned so = (unsigned)(((tile * 4 + ksp) * 3 + s) * 4 + wave) * 2048u;
#pragma unroll
        for (int n = 0; n < 2; ++n) px[s][n] = __builtin_amdgcn_raw_buffer_load_b128(rp, ln * 16 + n * 1024, so, SC1);
      }
    got |= now;
    if (got == 7u) break;
    seen = (unsigned)__ballot(__hip_atomic_load(f, RLX_AGENT) == tag);
    asm volatile("" : "+s"(seen));
  }
  unsigned pk[4][2];
#pragma unroll
  for (int n = 0; n < 4; ++n) {
    f32x4 v = c.acc[0][n];
#pragma unroll
    for (int s = 0; s < 3; ++s) {
      const unsigned lo = px[s][n >> 1][(n & 1) * 2], hi = px[s][n >> 1][(n & 1) * 2 + 1];
      v += __builtin_bit_cast(f32x4, (u32x4){lo << 16, lo & 0xFFFF0000u, hi << 16, hi & 0xFFFF0000u});
    }
    pk[n][0] = pk_bf16(v[0], v[1]);
    pk[n][1] = pk_bf16(v[2], v[3]);
  }
  const __amdgpu_buffer_rsrc_t ro = mk_rsrc(out, MT * DM * 2);
  const unsigned g = ln >> 4, row = tb * 128 + (wave >> 1) * 64 + ksp * 16 + (ln & 15);
  const unsigned vo = row * (DM * 2) + (cb * 128 + (wave & 1) * 64 + (g & 1) * 16 + (g >> 1) * 8) * 2;
#pragma unroll
  for (int h = 0; h < 2; ++h) {
    const auto s0 = __builtin_amdgcn_permlane16_swap(pk[2 * h][0], pk[2 * h + 1][0], false, false);
    const auto s1 = __builtin_amdgcn_permlane16_swap(pk[2 * h][1], pk[2 * h + 1][1], false, false);
    st16((u32x4){s0[0], s1[0], s0[1], s1[1]}, ro, vo + h * 64, 0);
  }
}

extern "C" void kernel_launch(void* const* d_in, const int* in_sizes, int n_in, void* d_out, int out_size, void* d_ws,
                              size_t ws_size, hipStream_t stream) {
  (void)hipFuncSetAttribute((const void*)ffn_fused, hipFuncAttributeMaxDynamicSharedMemorySize, LDS_BYTES);
  ffn_fused<<<256, 512, LDS_BYTES, stream>>>((const __hip_bfloat16*)d_in[0], (const __hip_bfloat16*)d_in[1],
                                             (const __hip_bfloat16*)d_in[2], (const __hip_bfloat16*)d_in[3], (char*)d_ws,
                                             (__hip_bfloat16*)d_out);
}
```

```cpp
#include <hip/hip_runtime.h>
#include <hip/hip_bf16.h>

typedef __bf16 bf16x8 __attribute__((ext_vector_type(8)));
typedef __bf16 bf16x4 __attribute__((ext_vector_type(4)));
typedef __bf16 bf16x2 __attribute__((ext_vector_type(2)));
typedef float f32x4 __attribute__((ext_vector_type(4)));
typedef float f32x2 __attribute__((ext_vector_type(2)));
typedef unsigned u32x4 __attribute__((ext_vector_type(4)));
typedef unsigned long long u64;
typedef u64 u64x4 __attribute__((ext_vector_type(4)));
typedef u64 u64x2 __attribute__((ext_vector_type(2)));
#define LAS __attribute__((address_space(3)))
#define GAS __attribute__((address_space(1)))
#define DI __device__ __forceinline__
#define SCHED() __builtin_amdgcn_sched_barrier(0)
#define CFENCE() asm volatile("" ::: "memory")
#define BAR() do { CFENCE(); __builtin_amdgcn_s_barrier(); CFENCE(); } while (0)
#define WAIT_VM(n) asm volatile("s_waitcnt vmcnt(%0)" ::"n"(n) : "memory")
#define VIS_LGKM0() __builtin_amdgcn_s_waitcnt(0xC07F)
#define VIS_VM0() __builtin_amdgcn_s_waitcnt(0x0F70)
#define RLX_AGENT __ATOMIC_RELAXED, __HIP_MEMORY_SCOPE_AGENT

constexpr int MT = 1024, DM = 1024, FF = 3584;
constexpr int KS32 = FF / 32;
constexpr unsigned ST1 = 49152;
constexpr unsigned P1B = 16384;
constexpr unsigned SL2 = 16384;
constexpr unsigned WB2 = 4 * SL2;
constexpr unsigned LDS_BYTES = 163840;
constexpr unsigned SPIN_CAP = 1u << 22;
constexpr unsigned CODE_BYTES = 15232;
constexpr int SC1 = 16;
#define PA 0
#define WB 14
#define WBD 0
#define WDLT 1000
constexpr size_t WS_H = 0;
constexpr size_t WS_PART = (size_t)MT * FF * 2;
constexpr size_t WS_FLAG = WS_PART + (size_t)64 * 4 * 3 * 8192;
constexpr size_t WS_XFLAG = WS_FLAG + 32768;
constexpr size_t WS_EPOCH = WS_XFLAG + 65536;

DI __amdgpu_buffer_rsrc_t mk_rsrc(const void* p, unsigned bytes) {
  return __builtin_amdgcn_make_buffer_rsrc((void*)p, 0, bytes, 0x00020000);
}
template <int AUX, int IMM = 0>
DI void dma16(__amdgpu_buffer_rsrc_t r, unsigned lds_off, unsigned voff, unsigned soff) {
  __builtin_amdgcn_raw_ptr_buffer_load_lds(r, (LAS void*)(size_t)lds_off, 16, voff, soff, IMM, AUX);
}
DI void touch(__amdgpu_buffer_rsrc_t r, unsigned lds_off, unsigned voff, unsigned soff) {
  __builtin_amdgcn_raw_ptr_buffer_load_lds(r, (LAS void*)(size_t)lds_off, 4, voff, soff, 0, 0);
}
DI void st16(u32x4 v, __amdgpu_buffer_rsrc_t r, unsigned voff, unsigned soff) {
  SCHED();
  __builtin_amdgcn_raw_buffer_store_b128(v, r, voff, soff, SC1);
  asm volatile("s_nop 1");
  SCHED();
}
DI bf16x8 lds_b128(unsigned addr) { return *(LAS bf16x8*)(size_t)addr; }
DI bf16x4 lds_tr(unsigned a) { return __builtin_amdgcn_ds_read_tr16_b64_v4bf16((LAS bf16x4*)(size_t)a); }
DI bf16x8 cat(bf16x4 lo, bf16x4 hi) { return __builtin_shufflevector(lo, hi, 0, 1, 2, 3, 4, 5, 6, 7); }
DI f32x4 mfma(bf16x8 a, bf16x8 b, f32x4 c) { return __builtin_amdgcn_mfma_f32_16x16x32_bf16(a, b, c, 0, 0, 0); }
DI unsigned pk_bf16(float lo, float hi) { return __builtin_bit_cast(unsigned, __builtin_convertvector((f32x2){lo, hi}, bf16x2)); }

struct P1C { bf16x8 X[8]; bf16x4 Wl[2][4], Wh[2][4]; f32x4 acc[8][4]; unsigned ax0, ax1, aw0, aw1; };

DI void p1_tile(P1C& c, int& slot) {
  unsigned delta = 0;
#pragma unroll
  for (int k = 0; k < 2; ++k) {
#pragma unroll
    for (int m = 0; m < 8; ++m) {
      if (k == 1 && m == 2) { VIS_LGKM0(); BAR(); SCHED(); }
      const unsigned ax = k ? c.ax0 : c.ax1;
      const int i = m - 2;
      const unsigned aw = ((i & 1) ? c.aw1 : c.aw0) + (i >> 1) * 16384 + (k ? 0 : 8192);
      c.acc[m][0] = mfma(cat(c.Wl[k][0], c.Wh[k][0]), c.X[m], c.acc[m][0]); SCHED();
      if (m >= 1 + k) { c.X[m - 1 - k] = lds_b128(ax + (m - 1 - k) * 2048); SCHED(); }
      if (k == 1 && m == 0) { c.ax1 += delta; asm volatile("" : "+v"(c.ax1)); SCHED(); }
      c.acc[m][1] = mfma(cat(c.Wl[k][1], c.Wh[k][1]), c.X[m], c.acc[m][1]); SCHED();
      if (m >= 2 && m < 6) { c.Wl[1 - k][i & 3] = lds_tr(aw); SCHED(); }
      if (k == 0 && m == 1) {
        asm volatile("" : "+s"(slot));
        delta = slot == 2 ? (unsigned)(-2 * (int)ST1) : ST1;
        asm volatile("" : "+s"(delta));
        SCHED();
      }
      if (k == 0 && m == 6) { c.ax0 += delta; asm volatile("" : "+v"(c.ax0)); SCHED(); }
      if (k == 0 && m == 7) { c.aw1 += delta; asm volatile("" : "+v"(c.aw1)); SCHED(); }
      c.acc[m][2] = mfma(cat(c.Wl[k][2], c.Wh[k][2]), c.X[m], c.acc[m][2]); SCHED();
      if (m >= 2 && m < 6) { c.Wh[1 - k][i & 3] = lds_tr(aw + 1024); SCHED(); }
      if (m == 7 && k == 1) { c.X[6] = lds_b128(ax + 6 * 2048); SCHED(); }
      if (k == 0 && m == 1) { slot = slot == 2 ? 0 : slot + 1; asm volatile("" : "+s"(slot)); SCHED(); }
      if (k == 0 && m == 6) { c.aw0 += delta; asm volatile("" : "+v"(c.aw0)); SCHED(); }
      c.acc[m][3] = mfma(cat(c.Wl[k][3], c.Wh[k][3]), c.X[m], c.acc[m][3]); SCHED();
      if (m == 7) { c.X[7] = lds_b128(ax + 7 * 2048); SCHED(); }
    }
  }
}

struct P2C { bf16x8 H[2][4]; bf16x4 Wl[2][4], Wh[2][4]; f32x4 acc[4][4]; unsigned ah, aw[4]; };

template <int B>
DI void p2_read(P2C& c, int e, unsigned ks) {
  if (e == 2 || e >= 9) { const int m = e == 2 ? 0 : e - 8; c.H[B][m] = lds_b128(c.ah + m * 2048 + ks * 1024); }
  else {
    const int f = e < 2 ? e : e - 1, n = f >> 1;
    if (f & 1) c.Wh[B][n] = lds_tr(c.aw[n] + ks * 8192 + 1024); else c.Wl[B][n] = lds_tr(c.aw[n] + ks * 8192);
  }
}
template <int HF, bool LAST = false>
DI void p1_half(P1C& c, int& slot, unsigned flip, P2C* c2 = nullptr) {
  unsigned delta = 0;
#pragma unroll
  for (int gp = 0; gp < 16; ++gp) {
    const int m = gp >> 1, f = (gp & 1) * 2;
    c.acc[m][HF + f] = mfma(cat(c.Wl[0][f], c.Wh[0][f]), c.X[m], c.acc[m][HF + f]); SCHED();
    if (gp == 0) { asm volatile("" : "+s"(slot)); delta = slot == 2 ? (unsigned)(-2 * (int)ST1) : ST1; asm volatile("" : "+s"(delta)); }
    if (gp >= 2 && !(gp & 1)) c.X[m - 1] = lds_b128(c.ax1 + (m - 1) * 2048);
    if (gp == 1) c.Wl[1][0] = lds_tr(c.aw0 + 8192);
    if (gp == 3) c.Wh[1][0] = lds_tr(c.aw0 + 8192 + 1024);
    if (gp == 5) c.Wl[1][2] = lds_tr(c.aw0 + 16384 + 8192);
    if (gp == 7) c.Wh[1][2] = lds_tr(c.aw0 + 16384 + 8192 + 1024);
    if (gp == 9) { c.aw0 = (c.aw0 + delta) ^ flip; asm volatile("" : "+v"(c.aw0)); }
    if (gp == 11) { c.ax0 += delta; asm volatile("" : "+v"(c.ax0)); }
    if (gp == 13) { slot = slot == 2 ? 0 : slot + 1; asm volatile("" : "+s"(slot)); }
    if (gp == 15) c.X[7] = lds_b128(c.ax1 + 7 * 2048);
    SCHED();
  }
#pragma unroll
  for (int gp = 0; gp < 16; ++gp) {
    const int m = gp >> 1, f = (gp & 1) * 2;
    if (gp == 6) { VIS_LGKM0(); BAR(); SCHED(); }
    c.acc[m][HF + f] = mfma(cat(c.Wl[1][f], c.Wh[1][f]), c.X[m], c.acc[m][HF + f]); SCHED();
    if (LAST) {
      if (gp >= 6) p2_read<0>(*c2, gp - 6, 0);
      if (gp >= 14) p2_read<0>(*c2, gp - 4, 0);
      SCHED();
      continue;
    }
    if (gp == 0) { c.ax1 += delta; asm volatile("" : "+v"(c.ax1)); }
    if (gp == 6) { c.Wl[0][0] = lds_tr(c.aw0); c.Wh[0][0] = lds_tr(c.aw0 + 1024); }
    if (gp == 7) c.X[0] = lds_b128(c.ax0);
    if (gp == 8) c.Wl[0][2] = lds_tr(c.aw0 + 16384);
    if (gp == 9) c.Wh[0][2] = lds_tr(c.aw0 + 16384 + 1024);
    if (gp >= 10) c.X[gp - 9] = lds_b128(c.ax0 + (gp - 9) * 2048);
    if (gp == 15) c.X[7] = lds_b128(c.ax0 + 7 * 2048);
    SCHED();
  }
}

template <int HF, bool DIRECT>
DI void p1_epi(P1C& c, unsigned la0, unsigned la2, __amdgpu_buffer_rsrc_t rh, unsigned vo0) {
  u32x4 o[4];
  f32x4 ta = c.acc[0][HF] * -1.44269504088896f, tb = c.acc[1][HF] * -1.44269504088896f;
  SCHED();
#pragma unroll
  for (int m = 0; m < 8; m += 2) {
#pragma unroll
    for (int i = 0; i < 4; ++i) ta[i] = __builtin_amdgcn_exp2f(ta[i]);
#pragma unroll
    for (int i = 0; i < 4; ++i) tb[i] = __builtin_amdgcn_exp2f(tb[i]);
    SCHED();
    f32x4 da = ta + 1.0f, db = tb + 1.0f;
    SCHED();
#pragma unroll
    for (int i = 0; i < 4; ++i) da[i] = __builtin_amdgcn_rcpf(da[i]);
#pragma unroll
    for (int i = 0; i < 4; ++i) db[i] = __builtin_amdgcn_rcpf(db[i]);
    SCHED();
    if (m < 6) { ta = c.acc[m + 2][HF] * -1.44269504088896f; tb = c.acc[m + 3][HF] * -1.44269504088896f; }
    unsigned vo = vo0 + m * (KS32 * 1024);
    if (DIRECT) asm volatile("" : "+v"(vo));
    SCHED();
    da *= c.acc[m][HF]; db *= c.acc[m + 1][HF];
    SCHED();
    da *= c.acc[m][HF + 2]; db *= c.acc[m + 1][HF + 2];
    SCHED();
    const auto s0 = __builtin_amdgcn_permlane16_swap(pk_bf16(da[0], da[1]), pk_bf16(db[0], db[1]), false, false);
    const auto s1 = __builtin_amdgcn_permlane16_swap(pk_bf16(da[2], da[3]), pk_bf16(db[2], db[3]), false, false);
    o[m >> 1] = (u32x4){s0[0], s1[0], s0[1], s1[1]};
    SCHED();
    if (DIRECT) __builtin_amdgcn_raw_buffer_store_b128(o[m >> 1], rh, vo, 0, SC1);
    *(LAS u32x4*)(size_t)(((m & 2) ? la2 : la0) + (m >> 2) * 8192) = o[m >> 1];
    SCHED();
  }
#pragma unroll
  for (int m = 0; m < 4; ++m) asm volatile("" ::"v"(o[m]));
}

template <bool FIRST>
DI void p2_tile(P2C& c, int& sw6, int& sh4) {
  unsigned dw = 0, dh = 0;
#pragma unroll
  for (int gp = 0; gp < 16; ++gp) {
    const int m = gp >> 2, n = gp & 3;
    c.acc[m][n] = mfma(cat(c.Wl[0][n], c.Wh[0][n]), c.H[0][m], FIRST ? (f32x4){0.f, 0.f, 0.f, 0.f} : c.acc[m][n]); SCHED();
    if (gp < 12) { p2_read<1>(c, gp, 1); SCHED(); }
    if (gp == 12) { asm volatile("" : "+s"(sw6)); dw = sw6 == 5 ? (unsigned)(-5 * (int)SL2) : SL2; asm volatile("" : "+s"(dw)); SCHED(); }
    if (gp == 13) { asm volatile("" : "+s"(sh4)); dh = sh4 == 3 ? (unsigned)(-3 * (int)SL2) : SL2; asm volatile("" : "+s"(dh)); SCHED(); }
    if (gp == 14) { c.aw[0] += dw; c.aw[1] += dw; asm volatile("" : "+v"(c.aw[0]), "+v"(c.aw[1])); SCHED(); }
    if (gp == 15) { c.aw[2] += dw; c.aw[3] += dw; asm volatile("" : "+v"(c.aw[2]), "+v"(c.aw[3])); SCHED(); }
  }
#pragma unroll
  for (int gp = 0; gp < 16; ++gp) {
    const int m = gp >> 2, n = gp & 3;
    if (gp == 4) { VIS_LGKM0(); BAR(); SCHED(); }
    c.acc[m][n] = mfma(cat(c.Wl[1][n], c.Wh[1][n]), c.H[1][m], c.acc[m][n]); SCHED();
    if (gp == 0) { c.ah += dh; asm volatile("" : "+v"(c.ah)); SCHED(); }
    if (gp == 1) { sw6 = sw6 == 5 ? 0 : sw6 + 1; asm volatile("" : "+s"(sw6)); SCHED(); }
    if (gp == 2) { sh4 = (sh4 + 1) & 3; asm volatile("" : "+s"(sh4)); SCHED(); }
    if (gp >= 4) { p2_read<0>(c, gp - 4, 0); SCHED(); }
  }
}

template <int B>
DI void p2_fread(P2C& c, int e, unsigned ks) {
  if (e == 2 || e >= 9) { const int m = e == 2 ? 1 : e - 7; c.H[B][m] = lds_b128(c.ah + m * 2048 + ks * 1024); }
  else p2_read<B>(c, e, ks);
}
DI void p2_ftile(P2C& c, int& sw6, int& sh4) {
  unsigned dw = 0, dh = 0;
#pragma unroll
  for (int gp = 0; gp < 12; ++gp) {
    const int m = 1 + (gp >> 2), n = gp & 3;
    c.acc[m][n] = mfma(cat(c.Wl[0][n], c.Wh[0][n]), c.H[0][m], c.acc[m][n]); SCHED();
    if (gp < 11) p2_fread<1>(c, gp, 1);
    if (gp == 5) { asm volatile("" : "+s"(sw6)); dw = sw6 == 5 ? (unsigned)(-5 * (int)SL2) : SL2; asm volatile("" : "+s"(dw)); }
    if (gp == 6) { asm volatile("" : "+s"(sh4)); dh = sh4 == 3 ? (unsigned)(-3 * (int)SL2) : SL2; asm volatile("" : "+s"(dh)); }
    if (gp == 7) { c.aw[0] += dw; asm volatile("" : "+v"(c.aw[0])); }
    if (gp == 9) { c.aw[1] += dw; asm volatile("" : "+v"(c.aw[1])); }
    if (gp == 10) { c.aw[2] += dw; asm volatile("" : "+v"(c.aw[2])); }
    if (gp == 11) { c.aw[3] += dw; c.ah += dh; asm volatile("" : "+v"(c.aw[3]), "+v"(c.ah)); }
    SCHED();
  }
  BAR();
  SCHED();
#pragma unroll
  for (int gp = 0; gp < 12; ++gp) {
    const int m = 1 + (gp >> 2), n = gp & 3;
    c.acc[m][n] = mfma(cat(c.Wl[1][n], c.Wh[1][n]), c.H[1][m], c.acc[m][n]); SCHED();
    if (gp < 11) p2_fread<0>(c, gp, 0);
    if (gp == 11) { sw6 = sw6 == 5 ? 0 : sw6 + 1; sh4 = (sh4 + 1) & 3; asm volatile("" : "+s"(sw6), "+s"(sh4)); }
    SCHED();
  }
}
DI void p2_otile(P2C& c, unsigned dw, unsigned dh) {
#pragma unroll
  for (int e = 0; e < 9; ++e) p2_read<0>(c, e, 0);
#pragma unroll
  for (int e = 0; e < 9; ++e) p2_read<1>(c, e, 1);
#pragma unroll
  for (int k = 0; k < 2; ++k)
#pragma unroll
    for (int n = 0; n < 4; ++n) c.acc[0][n] = mfma(cat(c.Wl[k][n], c.Wh[k][n]), c.H[k][0], c.acc[0][n]);
  c.ah += dh;
#pragma unroll
  for (int n = 0; n < 4; ++n) c.aw[n] += dw;
}

__global__ __launch_bounds__(512, 2) void ffn_fused(const __hip_bfloat16*, const __hip_bfloat16*, const __hip_bfloat16*,
                                                    const __hip_bfloat16*, char*, __hip_bfloat16*) {
  extern __shared__ __attribute__((aligned(16))) char smem[];
  u64x4 ka; u64x2 kb;
  asm volatile("s_load_dwordx8 %0, %2, 0x0\n\ts_load_dwordx4 %1, %2, 0x20" : "=&s"(ka), "=&s"(kb) : "s"(__builtin_amdgcn_kernarg_segment_ptr()));
  u64 pc0; asm volatile("s_getpc_b64 %0" : "=s"(pc0));
  const int bid = blockIdx.x, xcd = bid & 7, idx = bid >> 3;
  const int ksp = xcd >> 1, xe = xcd & 1, tb = idx >> 2, u = idx & 3;
  const bool spare = u == 3 && (tb >> 2) != xe;
  const int j = u < 3 ? xe * 4 + u : 3;
  const int fb = ksp * 7 + j, cb = xe * 4 + u;
  const int wave = __builtin_amdgcn_readfirstlane(threadIdx.x >> 6), l = threadIdx.x & 63;
  const unsigned L0 = (unsigned)(size_t)(LAS char*)smem;
  if (wave == 3) touch(mk_rsrc((const void*)(pc0 & ~(u64)127), CODE_BYTES), L0 + 8192 + 768, (idx + (l >> 4) * 32) * 128, 0);
  unsigned vx0, vw0, vp0;
  {
    const int xr = 8 * wave + (l >> 3);
    const unsigned sx = ((xr >> 1) & 3) | ((((xr >> 2) ^ (xr >> 3)) & 1) << 2);
    vx0 = xr * 2048 + (((l & 7) ^ sx) << 4);
    const unsigned sw = (l >> 4) | ((wave & 2) << 1);
    vw0 = (4 * wave + (l >> 4)) * (FF * 2) + (((((l & 15) >> 1) ^ sw)) << 5) + ((l & 1) << 4);
    vp0 = wave ? (idx * 32 + (l >> 1)) * 2048 + (l & 1) * 128
               : ((idx & 15) * 8 + (l >> 3)) * (FF * 2) + (ksp * 7 + xe * 3) * 256 + (l & 7) * 128;
  }
  asm volatile("s_waitcnt lgkmcnt(0)" : "+s"(ka), "+s"(kb), "+v"(vx0), "+v"(vw0), "+v"(vp0));
  const void *x = (const void*)ka.x, *wg = (const void*)ka.y, *wu = (const void*)ka.z, *wd = (const void*)ka.w;
  char* ws = (char*)kb.x;
  void* out = (void*)kb.y;
  u64 tag = __hip_atomic_load((GAS u64*)(ws + WS_EPOCH), RLX_AGENT);
  CFENCE();
  GAS u64* hflag = (GAS u64*)(ws + WS_FLAG) + (tb * 4 + ksp) * 128;
  auto unit_table = [&]() {
    const u64 a = (0xCA86420CA86420ull >> (4 * j)) & 0xFFFFFFFull, b = a | 0x1111111ull;
    return spare ? (a | (b << 28)) : ((a & 15ull) | ((b & 15ull) << 4) | ((a >> 4) << 8) | ((b >> 4) << 32));
  };
  const __amdgpu_buffer_rsrc_t rx = mk_rsrc(x, MT * DM * 2), rg = mk_rsrc(wg, DM * FF * 2), ru = mk_rsrc(wu, DM * FF * 2);
  if (wave < 2) touch(wave ? rx : ((idx & 16) ? ru : rg), L0 + 8192 + wave * 256, vp0, 0);
  if (__builtin_expect(!spare, 1)) {
    const unsigned sx0 = tb * 128 * 2048, sw0 = fb * 256, lb = L0 + P1B + wave * 1024;
#pragma unroll
    for (int a = 0; a < 2; ++a) {
      dma16<0>(rg, lb + 16384 + a * 8192, vw0, sw0 + a * 32 * (FF * 2));
      dma16<0>(ru, lb + 32768 + a * 8192, vw0, sw0 + a * 32 * (FF * 2));
      dma16<0>(rx, lb + a * 8192, vx0, sx0 + a * 64 * 2048);
    }
    if (wave >= 4) {
#pragma unroll
      for (int a = 0; a < 2; ++a) {
        dma16<0>(rg, lb + ST1 + 16384 + (a - 1) * 4096, vw0, sw0 + (64 + (a - 1) * 16) * (FF * 2));
        dma16<0>(ru, lb + ST1 + 32768 + (a - 1) * 4096, vw0, sw0 + (64 + (a - 1) * 16) * (FF * 2));
        dma16<0>(rx, lb + ST1 + (a - 1) * 4096, vx0 - 32 * 2048, sx0 + a * 32 * 2048 + 128);
      }
    }
  } else {
    const u64 t_start = __builtin_amdgcn_s_memrealtime();
    const int part = (tb & 3) * 4 + (wave & 3);
    const __amdgpu_buffer_rsrc_t rs = (part & 8) ? ru : rg;
    const unsigned vo = ((part & 7) * 8 + (l >> 3)) * (FF * 2) + (ksp * 7 + xe * 3) * 256 + (l & 7) * 128;
#pragma unroll 1
    for (int t = 2 + (wave >> 2), it = 2; t < 16; t += 2, ++it) {
      touch(rs, L0 + wave * 256, vo, t * 64 * (FF * 2));
      if (it == wave || (it == 2 && wave == 1))
        touch(rx, L0 + wave * 256, (xcd * 128 + (tb & 3) * 32 + (l & 31)) * 2048 + (wave * 2 + (l >> 5)) * 128, 0);
      WAIT_VM(1);
    }
    if (wave < 4 || wave >= 6) {
      const __amdgpu_buffer_rsrc_t rw = mk_rsrc(wd, FF * DM * 2);
      const unsigned vw = (ksp * 896 + (l >> 3)) * 2048 + xe * 1024 + (l & 7) * 128;
#pragma unroll 1
      for (int e = (tb & 3) * 6 + (wave < 4 ? wave : wave - 2); e < 112; e += 24) {
        if (__builtin_amdgcn_s_memrealtime() - t_start > WDLT) break;
        const int un = e >> 3, r = (un < 7 ? 2 * un : 2 * un - 13) * 8 + (e & 7);
        touch(rw, L0 + wave * 256, vw, r * 8 * 2048);
        WAIT_VM(1);
      }
    }
    VIS_VM0();
    CFENCE();
  }

  if (wave >= 6) {
    const int lw = wave - 6;
    if (!spare) { WAIT_VM(6); BAR(); }
    asm volatile("" : "+v"(tag));
    tag += 1ull;
    asm volatile("" : "+v"(tag));
    WAIT_VM(0);
    unsigned w0 = 0, w1 = 0;
    const u64 tbl = unit_table();
    GAS u64* fw;
    { const unsigned un = (unsigned)(tbl >> (4 * ((l >> 2) < 14 ? (l >> 2) : 13))) & 15u; fw = hflag + (un >> 1) * 16 + (un & 1) * 4 + (l & 3); }
    auto burst = [&]() {
      const __amdgpu_buffer_rsrc_t rw = mk_rsrc(wd, FF * DM * 2);
      const int me = idx - ((xe ? (tb < 4 ? tb : 4) : (tb > 4 ? tb - 4 : 0)));
      const unsigned vb = (ksp * 896 + me * 32 + lw * 16 + (l >> 3)) * 2048 + xe * 1024 + (l & 7) * 128;
      w0 = __builtin_amdgcn_raw_buffer_load_b32(rw, vb, 0, 0);
      w1 = __builtin_amdgcn_raw_buffer_load_b32(rw, vb, 8 * 2048, 0);
    };
    if (!spare) {
#pragma unroll 1
      for (int t = 1; t <= (WB < 15 ? WB : 15); ++t) BAR();
      if (WB < 16) { if (WBD) __builtin_amdgcn_s_sleep(WBD); burst(); }
#pragma unroll 1
      for (int t = WB + 1; t < 16; ++t) BAR();
    }
    BAR();
    const __amdgpu_buffer_rsrc_t rh = mk_rsrc(ws + WS_H, MT * FF * 2);
    unsigned tofs[4];
#pragma unroll
    for (int a = 0; a < 4; ++a) tofs[a] = (unsigned)((tb * 8 + lw * 4 + ((a + ksp) & 3)) * KS32 + ksp * 28) * 1024u;
    auto issue_h = [&](int kt) {
      const unsigned sb = ((unsigned)(tbl >> (4 * kt)) & 15u) * 2048u;
      const unsigned lb = L0 + (kt & 3) * SL2 + lw * 8192;
#pragma unroll
      for (int a = 0; a < 4; ++a) {
        dma16<SC1>(rh, lb + a * 2048, l * 16, sb + tofs[a]);
        dma16<SC1, 1024>(rh, lb + a * 2048, l * 16, sb + tofs[a]);
      }
    };
    int nrdy = 0;
    auto look = [&](u64 v) { nrdy = __builtin_ctzll(~(__ballot(v == tag) | (spare ? 0ull : 255ull)) | (1ull << 63)) >> 2; CFENCE(); };
    if (spare) {
      if (l == 0) __hip_atomic_store(hflag + 7 * 16 + lw, tag, RLX_AGENT);
#pragma unroll 1
      for (unsigned spins = 0; spins < SPIN_CAP && nrdy < 1; ++spins) { __builtin_amdgcn_s_sleep(8); look(__hip_atomic_load(fw, RLX_AGENT)); }
    } else {
      BAR(); if (WB == 17) burst(); BAR(); BAR();
#pragma unroll 1
      for (int i = 0; i < 3 && nrdy < 4; ++i) look(__hip_atomic_load(fw, RLX_AGENT));
    }
    VIS_VM0();
    asm volatile("" ::"v"(w0), "v"(w1));
    int nx = spare ? 0 : 2;
#pragma unroll 1
    for (int kt = spare ? 0 : 1; kt < 14; ++kt) {
      if (nx == kt + 2) {
        const int fe = nrdy - 3;
#pragma unroll 1
        for (; kt < fe; ++kt) { issue_h(kt + 2); WAIT_VM(16); BAR(); }
        nx = kt + 2;
      }
      const int lim = kt < 11 ? (kt ? kt + 2 : 3) : 13;
      if (__builtin_expect(kt >= nrdy, 0)) {
#pragma unroll 1
        for (unsigned spins = 0; spins < SPIN_CAP && kt >= nrdy; ++spins) look(__hip_atomic_load(fw, RLX_AGENT));
      }
      u64 pv;
      const bool pend = lim + 1 >= nrdy;
      if (__builtin_expect(pend, 0)) asm volatile("global_load_dwordx2 %0, %1, off sc1" : "=v"(pv) : "v"(fw) : "memory");
      const int n0 = nx;
#pragma unroll 1
      while (nx <= lim && nx < nrdy) { issue_h(nx); ++nx; }
      if (__builtin_expect(nx >= kt + 3, 1)) WAIT_VM(16); else if (nx == kt + 2) WAIT_VM(8); else WAIT_VM(0);
      BAR();
      if (__builtin_expect(pend, 0)) {
        if (nx > n0) WAIT_VM(8); else WAIT_VM(0);
        asm volatile("" : "+v"(pv));
        look(pv);
      }
    }
    BAR();
    if (bid == 0 && lw == 0) {
      GAS u64* fa = (GAS u64*)(ws + WS_FLAG);
#pragma unroll 1
      for (unsigned spins = 0; spins < SPIN_CAP; ++spins) {
        bool ok = true;
#pragma unroll
        for (int i = 0; i < 4; ++i) ok &= __hip_atomic_load(fa + (i * 64 + l) * 16, RLX_AGENT) == tag;
        if (__all(ok)) break;
        __builtin_amdgcn_s_sleep(2);
      }
      if (l == 0) __hip_atomic_store((GAS u64*)(ws + WS_EPOCH), tag, RLX_AGENT);
    }
    return;
  }
  if (wave >= 4) {
    const int lw = wave - 4;
    const __amdgpu_buffer_rsrc_t rw = mk_rsrc(wd, FF * DM * 2);
    const unsigned vd = (8 * lw + (l >> 4)) * (DM * 2) + (((((l & 15) >> 1) ^ ((l >> 4) | (lw << 2)))) << 5) + ((l & 1) << 4);
    const unsigned sd0 = cb * 256 + ksp * 896 * (DM * 2);
    const unsigned vd2 = vd + 4 * (DM * 2) - 1024;
    u64 tbl = 0;
    auto issue_w = [&](int kt, unsigned slot) {
      const unsigned sb = sd0 + ((unsigned)(tbl >> (4 * kt)) & 15u) * (64 * DM * 2);
      const unsigned lb = L0 + WB2 + slot * SL2 + lw * 2048;
#pragma unroll
      for (int a = 0; a < 4; ++a) {
        dma16<0>(rw, lb + a * 4096, vd, sb + a * 16 * (DM * 2));
        dma16<0, 1024>(rw, lb + a * 4096, vd2, sb + a * 16 * (DM * 2));
      }
    };
    if (!spare) {
      const int xr = 8 * lw + (l >> 3);
      const unsigned sx = ((xr >> 1) & 3) | ((((xr >> 2) ^ (xr >> 3)) & 1) << 2);
      const unsigned vx = xr * 2048 + (((l & 7) ^ sx) << 4);
      const unsigned sw = (l >> 4) | (lw << 2);
      const unsigned vw = (8 * lw + (l >> 4)) * (FF * 2) + (((((l & 15) >> 1) ^ sw)) << 5) + ((l & 1) << 4);
      const unsigned vw2 = vw + 4 * (FF * 2) - 1024, vx2 = vx + 32768 - 2048;
      unsigned lb = L0 + P1B + ST1 + 8192, so = fb * 256 + 96 * (FF * 2), sxo = tb * 128 * 2048 + 128 + 131072;
      int n = 3;
#pragma unroll 1
      for (int t = 0; t < 14; ++t) {
        if (t == 0) WAIT_VM(6); else if (t == 1) WAIT_VM(12); else WAIT_VM(24);
        BAR();
        const int lim = t ? 2 * t + 6 : 5;
#pragma unroll 1
        for (; n < lim; ++n) {
#pragma unroll
          for (int a = 0; a < 2; ++a) {
            dma16<0>(rg, lb + 16384 + a * 4096 + lw * 2048, vw, so + a * 16 * (FF * 2));
            dma16<0, 1024>(rg, lb + 16384 + a * 4096 + lw * 2048, vw2, so + a * 16 * (FF * 2));
            dma16<0>(ru, lb + 32768 + a * 4096 + lw * 2048, vw, so + a * 16 * (FF * 2));
            dma16<0, 1024>(ru, lb + 32768 + a * 4096 + lw * 2048, vw2, so + a * 16 * (FF * 2));
            dma16<0>(rx, lb + a * 4096 + lw * 1024, vx, sxo + a * 65536);
            dma16<0, 2048>(rx, lb + a * 4096 + lw * 1024, vx2, sxo + a * 65536);
          }
          so += 32 * (FF * 2);
          if (n & 1) { lb += ST1 - 8192; if (lb == L0 + P1B + 3 * ST1) lb = L0 + P1B; sxo += 128 - 131072; }
          else { lb += 8192; sxo += 131072; }
        }
      }
      WAIT_VM(24); BAR();
      WAIT_VM(0); BAR();
      tbl = unit_table();
      BAR();
      BAR();
      issue_w(0, 0);
      BAR();
      issue_w(1, 1);
      WAIT_VM(8); BAR();
#pragma unroll 1
      for (int kt = 2; kt < 4; ++kt) issue_w(kt, kt);
      WAIT_VM(16); BAR();
#pragma unroll 1
      for (int kt = 4; kt < 6; ++kt) issue_w(kt, kt);
      WAIT_VM(24); BAR();
      issue_w(6, 0);
    } else {
      tbl = unit_table();
#pragma unroll 1
      for (int kt = 0; kt < 6; ++kt) issue_w(kt, kt);
      BAR();
      WAIT_VM(0); BAR();
      BAR();
      BAR();
      issue_w(6, 0);
    }
    unsigned slot = 1;
#pragma unroll 1
    for (int kt = 3; kt < 10; ++kt) {
      WAIT_VM(24); BAR();
      issue_w(kt + 4, slot);
      slot = slot == 5 ? 0 : slot + 1;
    }
    WAIT_VM(24); BAR();
    WAIT_VM(16); BAR();
    WAIT_VM(8); BAR();
    WAIT_VM(0); BAR();
    BAR();
    return;
  }

  P1C c1;
  P2C c;
  unsigned la0, la2, vo0;
  {
    const int g = l >> 4, q = (l & 15) >> 2, p = l & 3, r15 = l & 15;
    const unsigned b = L0 + P1B;
    const unsigned sx = ((r15 >> 1) & 3) | ((((r15 >> 2) ^ (r15 >> 3)) & 1) << 2);
    c1.ax0 = b + r15 * 128 + ((g ^ sx) << 4);
    c1.ax1 = b + r15 * 128 + (((4 + g) ^ sx) << 4);
    const unsigned sw = q | ((g & 1) << 2);
    c1.aw0 = b + 16384 + (8 * g + q) * 256 + p * 8 + ((wave ^ sw) << 5);
    c1.aw1 = b + 16384 + (8 * g + q) * 256 + p * 8 + (((4 + wave) ^ sw) << 5);
    asm volatile("" : "+v"(c1.ax0), "+v"(c1.ax1), "+v"(c1.aw0), "+v"(c1.aw1));
    c.ah = L0 + (wave >> 1) * 8192 + l * 16;
#pragma unroll
    for (int n = 0; n < 4; ++n) c.aw[n] = L0 + WB2 + (8 * g + q) * 256 + p * 8 + (((4 * (wave & 1) + n) ^ sw) << 5);
    const unsigned g1 = g & 1, in = ((wave & 1) * 2 + (g >> 1)) * 256 + r15 * 16 + (wave >> 1) * 1024;
    la0 = L0 + ((g1 - ksp) & 3) * 2048 + in; la2 = L0 + ((g1 + 2 - ksp) & 3) * 2048 + in;
    vo0 = (unsigned)((tb * 8 + g1) * KS32 + fb * 4 + 2) * 1024u + in;
    asm volatile("" : "+v"(c.ah), "+v"(c.aw[0]), "+v"(c.aw[1]), "+v"(c.aw[2]), "+v"(c.aw[3]), "+v"(la0), "+v"(la2), "+v"(vo0));
  }
#pragma unroll
  for (int m = 0; m < 8; ++m)
#pragma unroll
    for (int n = 0; n < 4; ++n) { c1.acc[m][n] = (f32x4){0.f, 0.f, 0.f, 0.f}; asm volatile("" : "+v"(c1.acc[m][n])); }
  SCHED();
  asm volatile("" : "+v"(tag));
  tag += 1ull;
  tag = ((u64)(unsigned)__builtin_amdgcn_readfirstlane((unsigned)(tag >> 32)) << 32) | (unsigned)__builtin_amdgcn_readfirstlane((unsigned)tag);
  asm volatile("" : "+s"(tag));
  __builtin_amdgcn_s_waitcnt(0x0070);
  SCHED();
  if (!spare) {
    BAR();
#pragma unroll
    for (int i = 0; i < 4; ++i) {
      const unsigned a = ((i & 1) ? c1.aw1 : c1.aw0) + (i >> 1) * 16384;
      c1.Wl[0][i] = lds_tr(a); c1.Wh[0][i] = lds_tr(a + 1024);
      if (i == 0) c1.X[0] = lds_b128(c1.ax0);
    }
#pragma unroll
    for (int m = 1; m < 8; ++m) c1.X[m] = lds_b128(c1.ax0 + m * 2048);
    SCHED();
    int slot = 0;
#pragma unroll 1
    for (int t = 0; t < 13; ++t) p1_tile(c1, slot);
#pragma unroll 1
    for (int t = 0; t < 3; ++t) p1_half<0>(c1, slot, t == 2 ? 128u : 0u);
    const __amdgpu_buffer_rsrc_t rh = mk_rsrc(ws + WS_H, MT * FF * 2);
    p1_epi<0, true>(c1, la0, la2, rh, vo0 - 2048);
#pragma unroll 1
    for (int t = 0; t < 2; ++t) {
      p1_half<1>(c1, slot, 0u);
      if (t == PA) { WAIT_VM(0); if (l == 0) __hip_atomic_store(hflag + j * 16 + wave, tag, RLX_AGENT); }
    }
    p1_half<1, true>(c1, slot, 0u, &c);
    p1_epi<1, true>(c1, la0 + SL2, la2 + SL2, rh, vo0);
  } else {
    BAR();
    BAR();
#pragma unroll
    for (int e = 0; e < 12; ++e) p2_read<0>(c, e, 0);
    SCHED();
  }

  int tz = threadIdx.x; asm volatile("" : "+v"(tz));
  const int ln = tz & 63;
  int sw6 = 0, sh4 = 0;
  p2_tile<true>(c, sw6, sh4);
  if (!spare) {
    WAIT_VM(0);
    if (ln == 0) __hip_atomic_store(hflag + j * 16 + 4 + wave, tag, RLX_AGENT);
  }
#pragma unroll 1
  for (int kt = 1; kt < 10; ++kt) p2_tile<false>(c, sw6, sh4);
#pragma unroll 1
  for (int kt = 10; kt < 14; ++kt) p2_ftile(c, sw6, sh4);
  const int tile = tb * 8 + cb;
  const __amdgpu_buffer_rsrc_t rp = mk_rsrc(ws + WS_PART, 64u * 4 * 3 * 8192);
#pragma unroll
  for (int mm = 1; mm < 4; ++mm) {
    const unsigned so = (unsigned)(((tile * 4 + ((ksp + mm) & 3)) * 3 + (mm - 1)) * 4 + wave) * 2048u;
#pragma unroll
    for (int n = 0; n < 4; n += 2) {
      const f32x4 a = c.acc[mm][n], b = c.acc[mm][n + 1];
      st16((u32x4){pk_bf16(a[0], a[1]), pk_bf16(a[2], a[3]), pk_bf16(b[0], b[1]), pk_bf16(b[2], b[3])}, rp, ln * 16 + n * 512, so);
    }
  }
#pragma unroll
  for (int n = 0; n < 4; ++n) c.aw[n] += 2 * SL2;
#pragma unroll 1
  for (int kt = 10; kt < 14; ++kt) p2_otile(c, kt == 11 ? (unsigned)(-5 * (int)SL2) : SL2, kt == 11 ? (unsigned)(-3 * (int)SL2) : SL2);
  SCHED();
  GAS u64* xf = (GAS u64*)(ws + WS_XFLAG);
  GAS u64* f = xf + ((tile * 4 + ksp) * 4 + wave) * 8 + (ln < 3 ? ln : 0);
  u64 fl = __hip_atomic_load(f, RLX_AGENT);
  WAIT_VM(1);
  __hip_atomic_store(xf + ((tile * 4 + ((ksp + (ln % 3) + 1) & 3)) * 4 + wave) * 8 + (ln % 3), tag, RLX_AGENT);
  SCHED();
  asm volatile("" : "+v"(fl));
  unsigned seen = (unsigned)__ballot(fl == tag);
  asm volatile("" : "+s"(seen));
  u32x4 px[3][2];
  unsigned got = 0;
#pragma unroll 1
  for (unsigned spins = 0; spins < SPIN_CAP; ++spins) {
    const unsigned now = seen & 7u & ~got;
    CFENCE();
#pragma unroll
    for (int s = 0; s < 3; ++s)
      if (now & (1u << s)) {
        const unsigned so = (unsigned)(((tile * 4 + ksp) * 3 + s) * 4 + wave) * 2048u;
#pragma unroll
        for (int n = 0; n < 2; ++n) px[s][n] = __builtin_amdgcn_raw_buffer_load_b128(rp, ln * 16 + n * 1024, so, SC1);
      }
    got |= now;
    if (got == 7u) break;
    seen = (unsigned)__ballot(__hip_atomic_load(f, RLX_AGENT) == tag);
    asm volatile("" : "+s"(seen));
  }
  unsigned pk[4][2];
#pragma unroll
  for (int n = 0; n < 4; ++n) {
    f32x4 v = c.acc[0][n];
#pragma unroll
    for (int s = 0; s < 3; ++s) {
      const unsigned lo = px[s][n >> 1][(n & 1) * 2], hi = px[s][n >> 1][(n & 1) * 2 + 1];
      v += __builtin_bit_cast(f32x4, (u32x4){lo << 16, lo & 0xFFFF0000u, hi << 16, hi & 0xFFFF0000u});
    }
    pk[n][0] = pk_bf16(v[0], v[1]);
    pk[n][1] = pk_bf16(v[2], v[3]);
  }
  const __amdgpu_buffer_rsrc_t ro = mk_rsrc(out, MT * DM * 2);
  const unsigned g = ln >> 4, row = tb * 128 + (wave >> 1) * 64 + ksp * 16 + (ln & 15);
  const unsigned vo = row * (DM * 2) + (cb * 128 + (wave & 1) * 64 + (g & 1) * 16 + (g >> 1) * 8) * 2;
#pragma unroll
  for (int h = 0; h < 2; ++h) {
    const auto s0 = __builtin_amdgcn_permlane16_swap(pk[2 * h][0], pk[2 * h + 1][0], false, false);
    const auto s1 = __builtin_amdgcn_permlane16_swap(pk[2 * h][1], pk[2 * h + 1][1], false, false);
    st16((u32x4){s0[0], s1[0], s0[1], s1[1]}, ro, vo + h * 64, 0);
  }
}

extern "C" void kernel_launch(void* const* d_in, const int* in_sizes, int n_in, void* d_out, int out_size, void* d_ws,
                              size_t ws_size, hipStream_t stream) {
  (void)hipFuncSetAttribute((const void*)ffn_fused, hipFuncAttributeMaxDynamicSharedMemorySize, LDS_BYTES);
  ffn_fused<<<256, 512, LDS_BYTES, stream>>>((const __hip_bfloat16*)d_in[0], (const __hip_bfloat16*)d_in[1],
                                             (const __hip_bfloat16*)d_in[2], (const __hip_bfloat16*)d_in[3], (char*)d_ws,
                                             (__hip_bfloat16*)d_out);
}
```

```cpp
#include <hip/hip_runtime.h>
#include <hip/hip_bf16.h>

typedef __bf16 bf16x8 __attribute__((ext_vector_type(8)));
typedef __bf16 bf16x4 __attribute__((ext_vector_type(4)));
typedef __bf16 bf16x2 __attribute__((ext_vector_type(2)));
typedef float f32x4 __attribute__((ext_vector_type(4)));
typedef float f32x2 __attribute__((ext_vector_type(2)));
typedef unsigned u32x4 __attribute__((ext_vector_type(4)));
typedef unsigned long long u64;
typedef u64 u64x4 __attribute__((ext_vector_type(4)));
typedef u64 u64x2 __attribute__((ext_vector_type(2)));
#define LAS __attribute__((address_space(3)))
#define GAS __attribute__((address_space(1)))
#define DI __device__ __forceinline__
#define SCHED() __builtin_amdgcn_sched_barrier(0)
#define CFENCE() asm volatile("" ::: "memory")
#define BAR() do { CFENCE(); __builtin_amdgcn_s_barrier(); CFENCE(); } while (0)
#define WAIT_VM(n) asm volatile("s_waitcnt vmcnt(%0)" ::"n"(n) : "memory")
#define VIS_LGKM0() __builtin_amdgcn_s_waitcnt(0xC07F)
#define VIS_VM0() __builtin_amdgcn_s_waitcnt(0x0F70)
#define RLX_AGENT __ATOMIC_RELAXED, __HIP_MEMORY_SCOPE_AGENT

constexpr int MT = 1024, DM = 1024, FF = 3584;
constexpr int KS32 = FF / 32;
constexpr unsigned ST1 = 49152;
constexpr unsigned P1B = 16384;
constexpr unsigned SL2 = 16384;
constexpr unsigned WB2 = 4 * SL2;
constexpr unsigned LDS_BYTES = 163840;
constexpr unsigned SPIN_CAP = 1u << 22;
constexpr unsigned CODE_BYTES = 15232;
constexpr int SC1 = 16;
constexpr size_t WS_H = 0;
constexpr size_t WS_PART = (size_t)MT * FF * 2;
constexpr size_t WS_FLAG = WS_PART + (size_t)64 * 4 * 3 * 8192;
constexpr size_t WS_XFLAG = WS_FLAG + 32768;
constexpr size_t WS_EPOCH = WS_XFLAG + 65536;

DI __amdgpu_buffer_rsrc_t mk_rsrc(const void* p, unsigned bytes) {
  return __builtin_amdgcn_make_buffer_rsrc((void*)p, 0, bytes, 0x00020000);
}
template <int AUX, int IMM = 0>
DI void dma16(__amdgpu_buffer_rsrc_t r, unsigned lds_off, unsigned voff, unsigned soff) {
  __builtin_amdgcn_raw_ptr_buffer_load_lds(r, (LAS void*)(size_t)lds_off, 16, voff, soff, IMM, AUX);
}
DI void touch(__amdgpu_buffer_rsrc_t r, unsigned lds_off, unsigned voff, unsigned soff) {
  __builtin_amdgcn_raw_ptr_buffer_load_lds(r, (LAS void*)(size_t)lds_off, 4, voff, soff, 0, 0);
}
DI void st16(u32x4 v, __amdgpu_buffer_rsrc_t r, unsigned voff, unsigned soff) {
  SCHED();
  __builtin_amdgcn_raw_buffer_store_b128(v, r, voff, soff, SC1);
  asm volatile("s_nop 1");
  SCHED();
}
DI bf16x8 lds_b128(unsigned addr) { return *(LAS bf16x8*)(size_t)addr; }
DI bf16x4 lds_tr(unsigned a) { return __builtin_amdgcn_ds_read_tr16_b64_v4bf16((LAS bf16x4*)(size_t)a); }
DI bf16x8 cat(bf16x4 lo, bf16x4 hi) { return __builtin_shufflevector(lo, hi, 0, 1, 2, 3, 4, 5, 6, 7); }
DI f32x4 mfma(bf16x8 a, bf16x8 b, f32x4 c) { return __builtin_amdgcn_mfma_f32_16x16x32_bf16(a, b, c, 0, 0, 0); }
DI unsigned pk_bf16(float lo, float hi) { return __builtin_bit_cast(unsigned, __builtin_convertvector((f32x2){lo, hi}, bf16x2)); }

struct P1C { bf16x8 X[8]; bf16x4 Wl[2][4], Wh[2][4]; f32x4 acc[8][4]; unsigned ax0, ax1, aw0, aw1; };

DI void p1_tile(P1C& c, int& slot) {
  unsigned delta = 0;
#pragma unroll
  for (int k = 0; k < 2; ++k) {
#pragma unroll
    for (int m = 0; m < 8; ++m) {
      if (k == 1 && m == 2) { VIS_LGKM0(); BAR(); SCHED(); }
      const unsigned ax = k ? c.ax0 : c.ax1;
      const int i = m - 2;
      const unsigned aw = ((i & 1) ? c.aw1 : c.aw0) + (i >> 1) * 16384 + (k ? 0 : 8192);
      c.acc[m][0] = mfma(cat(c.Wl[k][0], c.Wh[k][0]), c.X[m], c.acc[m][0]); SCHED();
      if (m >= 1 + k) { c.X[m - 1 - k] = lds_b128(ax + (m - 1 - k) * 2048); SCHED(); }
      if (k == 1 && m == 0) { c.ax1 += delta; asm volatile("" : "+v"(c.ax1)); SCHED(); }
      c.acc[m][1] = mfma(cat(c.Wl[k][1], c.Wh[k][1]), c.X[m], c.acc[m][1]); SCHED();
      if (m >= 2 && m < 6) { c.Wl[1 - k][i & 3] = lds_tr(aw); SCHED(); }
      if (k == 0 && m == 1) {
        asm volatile("" : "+s"(slot));
        delta = slot == 2 ? (unsigned)(-2 * (int)ST1) : ST1;
        asm volatile("" : "+s"(delta));
        SCHED();
      }
      if (k == 0 && m == 6) { c.ax0 += delta; asm volatile("" : "+v"(c.ax0)); SCHED(); }
      if (k == 0 && m == 7) { c.aw1 += delta; asm volatile("" : "+v"(c.aw1)); SCHED(); }
      c.acc[m][2] = mfma(cat(c.Wl[k][2], c.Wh[k][2]), c.X[m], c.acc[m][2]); SCHED();
      if (m >= 2 && m < 6) { c.Wh[1 - k][i & 3] = lds_tr(aw + 1024); SCHED(); }
      if (m == 7 && k == 1) { c.X[6] = lds_b128(ax + 6 * 2048); SCHED(); }
      if (k == 0 && m == 1) { slot = slot == 2 ? 0 : slot + 1; asm volatile("" : "+s"(slot)); SCHED(); }
      if (k == 0 && m == 6) { c.aw0 += delta; asm volatile("" : "+v"(c.aw0)); SCHED(); }
      c.acc[m][3] = mfma(cat(c.Wl[k][3], c.Wh[k][3]), c.X[m], c.acc[m][3]); SCHED();
      if (m == 7) { c.X[7] = lds_b128(ax + 7 * 2048); SCHED(); }
    }
  }
}

struct P2C { bf16x8 H[2][4]; bf16x4 Wl[2][4], Wh[2][4]; f32x4 acc[4][4]; unsigned ah, aw[4]; };

template <int B>
DI void p2_read(P2C& c, int e, unsigned ks) {
  if (e == 2 || e >= 9) { const int m = e == 2 ? 0 : e - 8; c.H[B][m] = lds_b128(c.ah + m * 2048 + ks * 1024); }
  else {
    const int f = e < 2 ? e : e - 1, n = f >> 1;
    if (f & 1) c.Wh[B][n] = lds_tr(c.aw[n] + ks * 8192 + 1024); else c.Wl[B][n] = lds_tr(c.aw[n] + ks * 8192);
  }
}
template <int HF, bool LAST = false>
DI void p1_half(P1C& c, int& slot, unsigned flip, P2C* c2 = nullptr) {
  unsigned delta = 0;
#pragma unroll
  for (int gp = 0; gp < 16; ++gp) {
    const int m = gp >> 1, f = (gp & 1) * 2;
    c.acc[m][HF + f] = mfma(cat(c.Wl[0][f], c.Wh[0][f]), c.X[m], c.acc[m][HF + f]); SCHED();
    if (gp == 0) { asm volatile("" : "+s"(slot)); delta = slot == 2 ? (unsigned)(-2 * (int)ST1) : ST1; asm volatile("" : "+s"(delta)); }
    if (gp >= 2 && !(gp & 1)) c.X[m - 1] = lds_b128(c.ax1 + (m - 1) * 2048);
    if (gp == 1) c.Wl[1][0] = lds_tr(c.aw0 + 8192);
    if (gp == 3) c.Wh[1][0] = lds_tr(c.aw0 + 8192 + 1024);
    if (gp == 5) c.Wl[1][2] = lds_tr(c.aw0 + 16384 + 8192);
    if (gp == 7) c.Wh[1][2] = lds_tr(c.aw0 + 16384 + 8192 + 1024);
    if (gp == 9) { c.aw0 = (c.aw0 + delta) ^ flip; asm volatile("" : "+v"(c.aw0)); }
    if (gp == 11) { c.ax0 += delta; asm volatile("" : "+v"(c.ax0)); }
    if (gp == 13) { slot = slot == 2 ? 0 : slot + 1; asm volatile("" : "+s"(slot)); }
    if (gp == 15) c.X[7] = lds_b128(c.ax1 + 7 * 2048);
    SCHED();
  }
#pragma unroll
  for (int gp = 0; gp < 16; ++gp) {
    const int m = gp >> 1, f = (gp & 1) * 2;
    if (gp == 6) { VIS_LGKM0(); BAR(); SCHED(); }
    c.acc[m][HF + f] = mfma(cat(c.Wl[1][f], c.Wh[1][f]), c.X[m], c.acc[m][HF + f]); SCHED();
    if (LAST) {
      if (gp >= 6) p2_read<0>(*c2, gp - 6, 0);
      if (gp >= 14) p2_read<0>(*c2, gp - 4, 0);
      SCHED();
      continue;
    }
    if (gp == 0) { c.ax1 += delta; asm volatile("" : "+v"(c.ax1)); }
    if (gp == 6) { c.Wl[0][0] = lds_tr(c.aw0); c.Wh[0][0] = lds_tr(c.aw0 + 1024); }
    if (gp == 7) c.X[0] = lds_b128(c.ax0);
    if (gp == 8) c.Wl[0][2] = lds_tr(c.aw0 + 16384);
    if (gp == 9) c.Wh[0][2] = lds_tr(c.aw0 + 16384 + 1024);
    if (gp >= 10) c.X[gp - 9] = lds_b128(c.ax0 + (gp - 9) * 2048);
    if (gp == 15) c.X[7] = lds_b128(c.ax0 + 7 * 2048);
    SCHED();
  }
}

template <int HF>
DI void p1_epi(P1C& c, unsigned la0, unsigned la2, __amdgpu_buffer_rsrc_t rh, unsigned vo0) {
  u32x4 o[4];
  f32x4 ta = c.acc[0][HF] * -1.44269504088896f, tb = c.acc[1][HF] * -1.44269504088896f;
  SCHED();
#pragma unroll
  for (int m = 0; m < 8; m += 2) {
#pragma unroll
    for (int i = 0; i < 4; ++i) ta[i] = __builtin_amdgcn_exp2f(ta[i]);
#pragma unroll
    for (int i = 0; i < 4; ++i) tb[i] = __builtin_amdgcn_exp2f(tb[i]);
    SCHED();
    f32x4 da = ta + 1.0f, db = tb + 1.0f;
    SCHED();
#pragma unroll
    for (int i = 0; i < 4; ++i) da[i] = __builtin_amdgcn_rcpf(da[i]);
#pragma unroll
    for (int i = 0; i < 4; ++i) db[i] = __builtin_amdgcn_rcpf(db[i]);
    SCHED();
    if (m < 6) { ta = c.acc[m + 2][HF] * -1.44269504088896f; tb = c.acc[m + 3][HF] * -1.44269504088896f; }
    unsigned vo = vo0 + m * (KS32 * 1024);
    asm volatile("" : "+v"(vo));
    SCHED();
    da *= c.acc[m][HF]; db *= c.acc[m + 1][HF];
    SCHED();
    da *= c.acc[m][HF + 2]; db *= c.acc[m + 1][HF + 2];
    SCHED();
    const auto s0 = __builtin_amdgcn_permlane16_swap(pk_bf16(da[0], da[1]), pk_bf16(db[0], db[1]), false, false);
    const auto s1 = __builtin_amdgcn_permlane16_swap(pk_bf16(da[2], da[3]), pk_bf16(db[2], db[3]), false, false);
    o[m >> 1] = (u32x4){s0[0], s1[0], s0[1], s1[1]};
    SCHED();
    __builtin_amdgcn_raw_buffer_store_b128(o[m >> 1], rh, vo, 0, SC1);
    *(LAS u32x4*)(size_t)(((m & 2) ? la2 : la0) + (m >> 2) * 8192) = o[m >> 1];
    SCHED();
  }
#pragma unroll
  for (int m = 0; m < 4; ++m) asm volatile("" ::"v"(o[m]));
}

template <bool FIRST>
DI void p2_tile(P2C& c, int& sw6, int& sh4) {
  unsigned dw = 0, dh = 0;
#pragma unroll
  for (int gp = 0; gp < 16; ++gp) {
    const int m = gp >> 2, n = gp & 3;
    c.acc[m][n] = mfma(cat(c.Wl[0][n], c.Wh[0][n]), c.H[0][m], FIRST ? (f32x4){0.f, 0.f, 0.f, 0.f} : c.acc[m][n]); SCHED();
    if (gp < 12) { p2_read<1>(c, gp, 1); SCHED(); }
    if (gp == 12) { asm volatile("" : "+s"(sw6)); dw = sw6 == 5 ? (unsigned)(-5 * (int)SL2) : SL2; asm volatile("" : "+s"(dw)); SCHED(); }
    if (gp == 13) { asm volatile("" : "+s"(sh4)); dh = sh4 == 3 ? (unsigned)(-3 * (int)SL2) : SL2; asm volatile("" : "+s"(dh)); SCHED(); }
    if (gp == 14) { c.aw[0] += dw; c.aw[1] += dw; asm volatile("" : "+v"(c.aw[0]), "+v"(c.aw[1])); SCHED(); }
    if (gp == 15) { c.aw[2] += dw; c.aw[3] += dw; asm volatile("" : "+v"(c.aw[2]), "+v"(c.aw[3])); SCHED(); }
  }
#pragma unroll
  for (int gp = 0; gp < 16; ++gp) {
    const int m = gp >> 2, n = gp & 3;
    if (gp == 4) { VIS_LGKM0(); BAR(); SCHED(); }
    c.acc[m][n] = mfma(cat(c.Wl[1][n], c.Wh[1][n]), c.H[1][m], c.acc[m][n]); SCHED();
    if (gp == 0) { c.ah += dh; asm volatile("" : "+v"(c.ah)); SCHED(); }
    if (gp == 1) { sw6 = sw6 == 5 ? 0 : sw6 + 1; asm volatile("" : "+s"(sw6)); SCHED(); }
    if (gp == 2) { sh4 = (sh4 + 1) & 3; asm volatile("" : "+s"(sh4)); SCHED(); }
    if (gp >= 4) { p2_read<0>(c, gp - 4, 0); SCHED(); }
  }
}

template <int B>
DI void p2_fread(P2C& c, int e, unsigned ks) {
  if (e == 2 || e >= 9) { const int m = e == 2 ? 1 : e - 7; c.H[B][m] = lds_b128(c.ah + m * 2048 + ks * 1024); }
  else p2_read<B>(c, e, ks);
}
DI void p2_ftile(P2C& c, int& sw6, int& sh4) {
  unsigned dw = 0, dh = 0;
#pragma unroll
  for (int gp = 0; gp < 12; ++gp) {
    const int m = 1 + (gp >> 2), n = gp & 3;
    c.acc[m][n] = mfma(cat(c.Wl[0][n], c.Wh[0][n]), c.H[0][m], c.acc[m][n]); SCHED();
    if (gp < 11) p2_fread<1>(c, gp, 1);
    if (gp == 5) { asm volatile("" : "+s"(sw6)); dw = sw6 == 5 ? (unsigned)(-5 * (int)SL2) : SL2; asm volatile("" : "+s"(dw)); }
    if (gp == 6) { asm volatile("" : "+s"(sh4)); dh = sh4 == 3 ? (unsigned)(-3 * (int)SL2) : SL2; asm volatile("" : "+s"(dh)); }
    if (gp == 7) { c.aw[0] += dw; asm volatile("" : "+v"(c.aw[0])); }
    if (gp == 9) { c.aw[1] += dw; asm volatile("" : "+v"(c.aw[1])); }
    if (gp == 10) { c.aw[2] += dw; asm volatile("" : "+v"(c.aw[2])); }
    if (gp == 11) { c.aw[3] += dw; c.ah += dh; asm volatile("" : "+v"(c.aw[3]), "+v"(c.ah)); }
    SCHED();
  }
  BAR();
  SCHED();
#pragma unroll
  for (int gp = 0; gp < 12; ++gp) {
    const int m = 1 + (gp >> 2), n = gp & 3;
    c.acc[m][n] = mfma(cat(c.Wl[1][n], c.Wh[1][n]), c.H[1][m], c.acc[m][n]); SCHED();
    if (gp < 11) p2_fread<0>(c, gp, 0);
    if (gp == 11) { sw6 = sw6 == 5 ? 0 : sw6 + 1; sh4 = (sh4 + 1) & 3; asm volatile("" : "+s"(sw6), "+s"(sh4)); }
    SCHED();
  }
}
DI void p2_otile(P2C& c, unsigned dw, unsigned dh) {
#pragma unroll
  for (int e = 0; e < 9; ++e) p2_read<0>(c, e, 0);
#pragma unroll
  for (int e = 0; e < 9; ++e) p2_read<1>(c, e, 1);
#pragma unroll
  for (int k = 0; k < 2; ++k)
#pragma unroll
    for (int n = 0; n < 4; ++n) c.acc[0][n] = mfma(cat(c.Wl[k][n], c.Wh[k][n]), c.H[k][0], c.acc[0][n]);
  c.ah += dh;
#pragma unroll
  for (int n = 0; n < 4; ++n) c.aw[n] += dw;
}

__global__ __launch_bounds__(512, 2) void ffn_fused(const __hip_bfloat16*, const __hip_bfloat16*, const __hip_bfloat16*,
                                                    const __hip_bfloat16*, char*, __hip_bfloat16*) {
  extern __shared__ __attribute__((aligned(16))) char smem[];
  u64x4 ka; u64x2 kb;
  asm volatile("s_load_dwordx8 %0, %2, 0x0\n\ts_load_dwordx4 %1, %2, 0x20" : "=&s"(ka), "=&s"(kb) : "s"(__builtin_amdgcn_kernarg_segment_ptr()));
  u64 pc0; asm volatile("s_getpc_b64 %0" : "=s"(pc0));
  const int bid = blockIdx.x, xcd = bid & 7, idx = bid >> 3;
  const int ksp = xcd >> 1, xe = xcd & 1, tb = idx >> 2, u = idx & 3;
  const bool spare = u == 3 && (tb >> 2) != xe;
  const int j = u < 3 ? xe * 4 + u : 3;
  const int fb = ksp * 7 + j, cb = xe * 4 + u;
  const int wave = __builtin_amdgcn_readfirstlane(threadIdx.x >> 6), l = threadIdx.x & 63;
  const unsigned L0 = (unsigned)(size_t)(LAS char*)smem;
  if (wave == 3) touch(mk_rsrc((const void*)(pc0 & ~(u64)127), CODE_BYTES), L0 + 8192 + 768, (idx + (l >> 4) * 32) * 128, 0);
  unsigned vx0, vw0, vp0;
  {
    const int xr = 8 * wave + (l >> 3);
    const unsigned sx = ((xr >> 1) & 3) | ((((xr >> 2) ^ (xr >> 3)) & 1) << 2);
    vx0 = xr * 2048 + (((l & 7) ^ sx) << 4);
    const unsigned sw = (l >> 4) | ((wave & 2) << 1);
    vw0 = (4 * wave + (l >> 4)) * (FF * 2) + (((((l & 15) >> 1) ^ sw)) << 5) + ((l & 1) << 4);
    vp0 = wave ? (idx * 32 + (l >> 1)) * 2048 + (l & 1) * 128
               : ((idx & 15) * 8 + (l >> 3)) * (FF * 2) + (ksp * 7 + xe * 3) * 256 + (l & 7) * 128;
  }
  asm volatile("s_waitcnt lgkmcnt(0)" : "+s"(ka), "+s"(kb), "+v"(vx0), "+v"(vw0), "+v"(vp0));
  const void *x = (const void*)ka.x, *wg = (const void*)ka.y, *wu = (const void*)ka.z, *wd = (const void*)ka.w;
  char* ws = (char*)kb.x;
  void* out = (void*)kb.y;
  u64 tag = __hip_atomic_load((GAS u64*)(ws + WS_EPOCH), RLX_AGENT);
  CFENCE();
  GAS u64* hflag = (GAS u64*)(ws + WS_FLAG) + (tb * 4 + ksp) * 128;
  auto unit_table = [&]() {
    const u64 a = (0xCA86420CA86420ull >> (4 * j)) & 0xFFFFFFFull, b = a | 0x1111111ull;
    return spare ? (a | (b << 28)) : ((a & 15ull) | ((b & 15ull) << 4) | ((a >> 4) << 8) | ((b >> 4) << 32));
  };
  const __amdgpu_buffer_rsrc_t rx = mk_rsrc(x, MT * DM * 2), rg = mk_rsrc(wg, DM * FF * 2), ru = mk_rsrc(wu, DM * FF * 2);
  if (wave < 2) touch(wave ? rx : ((idx & 16) ? ru : rg), L0 + 8192 + wave * 256, vp0, 0);
  if (__builtin_expect(!spare, 1)) {
    const unsigned sx0 = tb * 128 * 2048, sw0 = fb * 256, lb = L0 + P1B + wave * 1024;
#pragma unroll
    for (int a = 0; a < 2; ++a) {
      dma16<0>(rg, lb + 16384 + a * 8192, vw0, sw0 + a * 32 * (FF * 2));
      dma16<0>(ru, lb + 32768 + a * 8192, vw0, sw0 + a * 32 * (FF * 2));
      dma16<0>(rx, lb + a * 8192, vx0, sx0 + a * 64 * 2048);
    }
    if (wave >= 4) {
#pragma unroll
      for (int a = 0; a < 2; ++a) {
        dma16<0>(rg, lb + ST1 + 16384 + (a - 1) * 4096, vw0, sw0 + (64 + (a - 1) * 16) * (FF * 2));
        dma16<0>(ru, lb + ST1 + 32768 + (a - 1) * 4096, vw0, sw0 + (64 + (a - 1) * 16) * (FF * 2));
        dma16<0>(rx, lb + ST1 + (a - 1) * 4096, vx0 - 32 * 2048, sx0 + a * 32 * 2048 + 128);
      }
    }
  } else {
    const u64 t_start = __builtin_amdgcn_s_memrealtime();
    const int part = (tb & 3) * 4 + (wave & 3);
    const __amdgpu_buffer_rsrc_t rs = (part & 8) ? ru : rg;
    const unsigned vo = ((part & 7) * 8 + (l >> 3)) * (FF * 2) + (ksp * 7 + xe * 3) * 256 + (l & 7) * 128;
#pragma unroll 1
    for (int t = 2 + (wave >> 2), it = 2; t < 16; t += 2, ++it) {
      touch(rs, L0 + wave * 256, vo, t * 64 * (FF * 2));
      if (it == wave || (it == 2 && wave == 1))
        touch(rx, L0 + wave * 256, (xcd * 128 + (tb & 3) * 32 + (l & 31)) * 2048 + (wave * 2 + (l >> 5)) * 128, 0);
      WAIT_VM(1);
    }
    if (wave < 4 || wave >= 6) {
      const __amdgpu_buffer_rsrc_t rw = mk_rsrc(wd, FF * DM * 2);
      const unsigned vw = (ksp * 896 + (l >> 3)) * 2048 + xe * 1024 + (l & 7) * 128;
#pragma unroll 1
      for (int e = (tb & 3) * 6 + (wave < 4 ? wave : wave - 2); e < 112; e += 24) {
        if (__builtin_amdgcn_s_memrealtime() - t_start > 1000) break;
        const int un = e >> 3, r = (un < 7 ? 2 * un : 2 * un - 13) * 8 + (e & 7);
        touch(rw, L0 + wave * 256, vw, r * 8 * 2048);
        WAIT_VM(1);
      }
    }
    VIS_VM0();
    CFENCE();
  }

  if (wave >= 6) {
    const int lw = wave - 6;
    if (!spare) { WAIT_VM(6); BAR(); }
    asm volatile("" : "+v"(tag));
    tag += 1ull;
    asm volatile("" : "+v"(tag));
    WAIT_VM(0);
    unsigned w0 = 0, w1 = 0;
    const u64 tbl = unit_table();
    GAS u64* fw;
    { const unsigned un = (unsigned)(tbl >> (4 * ((l >> 2) < 14 ? (l >> 2) : 13))) & 15u; fw = hflag + (un >> 1) * 16 + (un & 1) * 4 + (l & 3); }
    if (!spare) {
#pragma unroll 1
      for (int t = 1; t < 15; ++t) BAR();
      const __amdgpu_buffer_rsrc_t rw = mk_rsrc(wd, FF * DM * 2);
      const int me = idx - ((xe ? (tb < 4 ? tb : 4) : (tb > 4 ? tb - 4 : 0)));
      const unsigned vb = (ksp * 896 + me * 32 + lw * 16 + (l >> 3)) * 2048 + xe * 1024 + (l & 7) * 128;
      w0 = __builtin_amdgcn_raw_buffer_load_b32(rw, vb, 0, 0);
      w1 = __builtin_amdgcn_raw_buffer_load_b32(rw, vb, 8 * 2048, 0);
      BAR();
    }
    BAR();
    const __amdgpu_buffer_rsrc_t rh = mk_rsrc(ws + WS_H, MT * FF * 2);
    unsigned tofs[4];
#pragma unroll
    for (int a = 0; a < 4; ++a) tofs[a] = (unsigned)((tb * 8 + lw * 4 + ((a + ksp) & 3)) * KS32 + ksp * 28) * 1024u;
    auto issue_h = [&](int kt) {
      const unsigned sb = ((unsigned)(tbl >> (4 * kt)) & 15u) * 2048u;
      const unsigned lb = L0 + (kt & 3) * SL2 + lw * 8192;
#pragma unroll
      for (int a = 0; a < 4; ++a) {
        dma16<SC1>(rh, lb + a * 2048, l * 16, sb + tofs[a]);
        dma16<SC1, 1024>(rh, lb + a * 2048, l * 16, sb + tofs[a]);
      }
    };
    int nrdy = 0;
    auto look = [&](u64 v) { nrdy = __builtin_ctzll(~(__ballot(v == tag) | (spare ? 0ull : 255ull)) | (1ull << 63)) >> 2; CFENCE(); };
    if (spare) {
      if (l == 0) __hip_atomic_store(hflag + 7 * 16 + lw, tag, RLX_AGENT);
#pragma unroll 1
      for (unsigned spins = 0; spins < SPIN_CAP && nrdy < 1; ++spins) { __builtin_amdgcn_s_sleep(8); look(__hip_atomic_load(fw, RLX_AGENT)); }
    } else {
      BAR(); BAR(); BAR();
#pragma unroll 1
      for (int i = 0; i < 3 && nrdy < 4; ++i) look(__hip_atomic_load(fw, RLX_AGENT));
    }
    VIS_VM0();
    asm volatile("" ::"v"(w0), "v"(w1));
    int nx = spare ? 0 : 2;
#pragma unroll 1
    for (int kt = spare ? 0 : 1; kt < 14; ++kt) {
      if (nx == kt + 2) {
        const int fe = nrdy - 3;
#pragma unroll 1
        for (; kt < fe; ++kt) { issue_h(kt + 2); WAIT_VM(16); BAR(); }
        nx = kt + 2;
      }
      const int lim = kt < 11 ? (kt ? kt + 2 : 3) : 13;
      if (__builtin_expect(kt >= nrdy, 0)) {
#pragma unroll 1
        for (unsigned spins = 0; spins < SPIN_CAP && kt >= nrdy; ++spins) look(__hip_atomic_load(fw, RLX_AGENT));
      }
      u64 pv;
      const bool pend = lim + 1 >= nrdy;
      if (__builtin_expect(pend, 0)) asm volatile("global_load_dwordx2 %0, %1, off sc1" : "=v"(pv) : "v"(fw) : "memory");
      const int n0 = nx;
#pragma unroll 1
      while (nx <= lim && nx < nrdy) { issue_h(nx); ++nx; }
      if (__builtin_expect(nx >= kt + 3, 1)) WAIT_VM(16); else if (nx == kt + 2) WAIT_VM(8); else WAIT_VM(0);
      BAR();
      if (__builtin_expect(pend, 0)) {
        if (nx > n0) WAIT_VM(8); else WAIT_VM(0);
        asm volatile("" : "+v"(pv));
        look(pv);
      }
    }
    BAR();
    if (bid == 0 && lw == 0) {
      GAS u64* fa = (GAS u64*)(ws + WS_FLAG);
#pragma unroll 1
      for (unsigned spins = 0; spins < SPIN_CAP; ++spins) {
        bool ok = true;
#pragma unroll
        for (int i = 0; i < 4; ++i) ok &= __hip_atomic_load(fa + (i * 64 + l) * 16, RLX_AGENT) == tag;
        if (__all(ok)) break;
        __builtin_amdgcn_s_sleep(2);
      }
      if (l == 0) __hip_atomic_store((GAS u64*)(ws + WS_EPOCH), tag, RLX_AGENT);
    }
    return;
  }
  if (wave >= 4) {
    const int lw = wave - 4;
    const __amdgpu_buffer_rsrc_t rw = mk_rsrc(wd, FF * DM * 2);
    const unsigned vd = (8 * lw + (l >> 4)) * (DM * 2) + (((((l & 15) >> 1) ^ ((l >> 4) | (lw << 2)))) << 5) + ((l & 1) << 4);
    const unsigned sd0 = cb * 256 + ksp * 896 * (DM * 2);
    const unsigned vd2 = vd + 4 * (DM * 2) - 1024;
    u64 tbl = 0;
    auto issue_w = [&](int kt, unsigned slot) {
      const unsigned sb = sd0 + ((unsigned)(tbl >> (4 * kt)) & 15u) * (64 * DM * 2);
      const unsigned lb = L0 + WB2 + slot * SL2 + lw * 2048;
#pragma unroll
      for (int a = 0; a < 4; ++a) {
        dma16<0>(rw, lb + a * 4096, vd, sb + a * 16 * (DM * 2));
        dma16<0, 1024>(rw, lb + a * 4096, vd2, sb + a * 16 * (DM * 2));
      }
    };
    if (!spare) {
      const int xr = 8 * lw + (l >> 3);
      const unsigned sx = ((xr >> 1) & 3) | ((((xr >> 2) ^ (xr >> 3)) & 1) << 2);
      const unsigned vx = xr * 2048 + (((l & 7) ^ sx) << 4);
      const unsigned sw = (l >> 4) | (lw << 2);
      const unsigned vw = (8 * lw + (l >> 4)) * (FF * 2) + (((((l & 15) >> 1) ^ sw)) << 5) + ((l & 1) << 4);
      const unsigned vw2 = vw + 4 * (FF * 2) - 1024, vx2 = vx + 32768 - 2048;
      unsigned lb = L0 + P1B + ST1 + 8192, so = fb * 256 + 96 * (FF * 2), sxo = tb * 128 * 2048 + 128 + 131072;
      int n = 3;
#pragma unroll 1
      for (int t = 0; t < 14; ++t) {
        if (t == 0) WAIT_VM(6); else if (t == 1) WAIT_VM(12); else WAIT_VM(24);
        BAR();
        const int lim = t ? 2 * t + 6 : 5;
#pragma unroll 1
        for (; n < lim; ++n) {
#pragma unroll
          for (int a = 0; a < 2; ++a) {
            dma16<0>(rg, lb + 16384 + a * 4096 + lw * 2048, vw, so + a * 16 * (FF * 2));
            dma16<0, 1024>(rg, lb + 16384 + a * 4096 + lw * 2048, vw2, so + a * 16 * (FF * 2));
            dma16<0>(ru, lb + 32768 + a * 4096 + lw * 2048, vw, so + a * 16 * (FF * 2));
            dma16<0, 1024>(ru, lb + 32768 + a * 4096 + lw * 2048, vw2, so + a * 16 * (FF * 2));
            dma16<0>(rx, lb + a * 4096 + lw * 1024, vx, sxo + a * 65536);
            dma16<0, 2048>(rx, lb + a * 4096 + lw * 1024, vx2, sxo + a * 65536);
          }
          so += 32 * (FF * 2);
          if (n & 1) { lb += ST1 - 8192; if (lb == L0 + P1B + 3 * ST1) lb = L0 + P1B; sxo += 128 - 131072; }
          else { lb += 8192; sxo += 131072; }
        }
      }
      WAIT_VM(24); BAR();
      WAIT_VM(0); BAR();
      tbl = unit_table();
      BAR();
      BAR();
      issue_w(0, 0);
      BAR();
      issue_w(1, 1);
      WAIT_VM(8); BAR();
#pragma unroll 1
      for (int kt = 2; kt < 4; ++kt) issue_w(kt, kt);
      WAIT_VM(16); BAR();
#pragma unroll 1
      for (int kt = 4; kt < 6; ++kt) issue_w(kt, kt);
      WAIT_VM(24); BAR();
      issue_w(6, 0);
    } else {
      tbl = unit_table();
#pragma unroll 1
      for (int kt = 0; kt < 6; ++kt) issue_w(kt, kt);
      BAR();
      WAIT_VM(0); BAR();
      BAR();
      BAR();
      issue_w(6, 0);
    }
    unsigned slot = 1;
#pragma unroll 1
    for (int kt = 3; kt < 10; ++kt) {
      WAIT_VM(24); BAR();
      issue_w(kt + 4, slot);
      slot = slot == 5 ? 0 : slot + 1;
    }
    WAIT_VM(24); BAR();
    WAIT_VM(16); BAR();
    WAIT_VM(8); BAR();
    WAIT_VM(0); BAR();
    BAR();
    return;
  }

  P1C c1;
  P2C c;
  unsigned la0, la2, vo0;
  {
    const int g = l >> 4, q = (l & 15) >> 2, p = l & 3, r15 = l & 15;
    const unsigned b = L0 + P1B;
    const unsigned sx = ((r15 >> 1) & 3) | ((((r15 >> 2) ^ (r15 >> 3)) & 1) << 2);
    c1.ax0 = b + r15 * 128 + ((g ^ sx) << 4);
    c1.ax1 = b + r15 * 128 + (((4 + g) ^ sx) << 4);
    const unsigned sw = q | ((g & 1) << 2);
    c1.aw0 = b + 16384 + (8 * g + q) * 256 + p * 8 + ((wave ^ sw) << 5);
    c1.aw1 = b + 16384 + (8 * g + q) * 256 + p * 8 + (((4 + wave) ^ sw) << 5);
    asm volatile("" : "+v"(c1.ax0), "+v"(c1.ax1), "+v"(c1.aw0), "+v"(c1.aw1));
    c.ah = L0 + (wave >> 1) * 8192 + l * 16;
#pragma unroll
    for (int n = 0; n < 4; ++n) c.aw[n] = L0 + WB2 + (8 * g + q) * 256 + p * 8 + (((4 * (wave & 1) + n) ^ sw) << 5);
    const unsigned g1 = g & 1, in = ((wave & 1) * 2 + (g >> 1)) * 256 + r15 * 16 + (wave >> 1) * 1024;
    la0 = L0 + ((g1 - ksp) & 3) * 2048 + in; la2 = L0 + ((g1 + 2 - ksp) & 3) * 2048 + in;
    vo0 = (unsigned)((tb * 8 + g1) * KS32 + fb * 4 + 2) * 1024u + in;
    asm volatile("" : "+v"(c.ah), "+v"(c.aw[0]), "+v"(c.aw[1]), "+v"(c.aw[2]), "+v"(c.aw[3]), "+v"(la0), "+v"(la2), "+v"(vo0));
  }
#pragma unroll
  for (int m = 0; m < 8; ++m)
#pragma unroll
    for (int n = 0; n < 4; ++n) { c1.acc[m][n] = (f32x4){0.f, 0.f, 0.f, 0.f}; asm volatile("" : "+v"(c1.acc[m][n])); }
  SCHED();
  asm volatile("" : "+v"(tag));
  tag += 1ull;
  tag = ((u64)(unsigned)__builtin_amdgcn_readfirstlane((unsigned)(tag >> 32)) << 32) | (unsigned)__builtin_amdgcn_readfirstlane((unsigned)tag);
  asm volatile("" : "+s"(tag));
  __builtin_amdgcn_s_waitcnt(0x0070);
  SCHED();
  if (!spare) {
    BAR();
#pragma unroll
    for (int i = 0; i < 4; ++i) {
      const unsigned a = ((i & 1) ? c1.aw1 : c1.aw0) + (i >> 1) * 16384;
      c1.Wl[0][i] = lds_tr(a); c1.Wh[0][i] = lds_tr(a + 1024);
      if (i == 0) c1.X[0] = lds_b128(c1.ax0);
    }
#pragma unroll
    for (int m = 1; m < 8; ++m) c1.X[m] = lds_b128(c1.ax0 + m * 2048);
    SCHED();
    int slot = 0;
#pragma unroll 1
    for (int t = 0; t < 13; ++t) p1_tile(c1, slot);
#pragma unroll 1
    for (int t = 0; t < 3; ++t) p1_half<0>(c1, slot, t == 2 ? 128u : 0u);
    const __amdgpu_buffer_rsrc_t rh = mk_rsrc(ws + WS_H, MT * FF * 2);
    p1_epi<0>(c1, la0, la2, rh, vo0 - 2048);
#pragma unroll 1
    for (int t = 0; t < 2; ++t) {
      p1_half<1>(c1, slot, 0u);
      if (t == 0) { WAIT_VM(0); if (l == 0) __hip_atomic_store(hflag + j * 16 + wave, tag, RLX_AGENT); }
    }
    p1_half<1, true>(c1, slot, 0u, &c);
    p1_epi<1>(c1, la0 + SL2, la2 + SL2, rh, vo0);
  } else {
    BAR();
    BAR();
#pragma unroll
    for (int e = 0; e < 12; ++e) p2_read<0>(c, e, 0);
    SCHED();
  }

  int tz = threadIdx.x; asm volatile("" : "+v"(tz));
  const int ln = tz & 63;
  int sw6 = 0, sh4 = 0;
  p2_tile<true>(c, sw6, sh4);
  if (!spare) {
    WAIT_VM(0);
    if (ln == 0) __hip_atomic_store(hflag + j * 16 + 4 + wave, tag, RLX_AGENT);
  }
#pragma unroll 1
  for (int kt = 1; kt < 10; ++kt) p2_tile<false>(c, sw6, sh4);
#pragma unroll 1
  for (int kt = 10; kt < 14; ++kt) p2_ftile(c, sw6, sh4);
  const int tile = tb * 8 + cb;
  const __amdgpu_buffer_rsrc_t rp = mk_rsrc(ws + WS_PART, 64u * 4 * 3 * 8192);
#pragma unroll
  for (int mm = 1; mm < 4; ++mm) {
    const unsigned so = (unsigned)(((tile * 4 + ((ksp + mm) & 3)) * 3 + (mm - 1)) * 4 + wave) * 2048u;
#pragma unroll
    for (int n = 0; n < 4; n += 2) {
      const f32x4 a = c.acc[mm][n], b = c.acc[mm][n + 1];
      st16((u32x4){pk_bf16(a[0], a[1]), pk_bf16(a[2], a[3]), pk_bf16(b[0], b[1]), pk_bf16(b[2], b[3])}, rp, ln * 16 + n * 512, so);
    }
  }
#pragma unroll
  for (int n = 0; n < 4; ++n) c.aw[n] += 2 * SL2;
#pragma unroll 1
  for (int kt = 10; kt < 14; ++kt) p2_otile(c, kt == 11 ? (unsigned)(-5 * (int)SL2) : SL2, kt == 11 ? (unsigned)(-3 * (int)SL2) : SL2);
  SCHED();
  GAS u64* xf = (GAS u64*)(ws + WS_XFLAG);
  GAS u64* f = xf + ((tile * 4 + ksp) * 4 + wave) * 8 + (ln < 3 ? ln : 0);
  u64 fl = __hip_atomic_load(f, RLX_AGENT);
  WAIT_VM(1);
  {
    typedef unsigned u32x2 __attribute__((ext_vector_type(2)));
    const unsigned fo = ln < 3 ? (unsigned)(((tile * 4 + ((ksp + ln + 1) & 3)) * 4 + wave) * 8 + ln) * 8u : 0x80000000u;
    __builtin_amdgcn_raw_buffer_store_b64((u32x2){(unsigned)tag, (unsigned)(tag >> 32)}, mk_rsrc(ws + WS_XFLAG, 65536), fo, 0, SC1);
  }
  SCHED();
  asm volatile("" : "+v"(fl));
  unsigned seen = (unsigned)__ballot(fl == tag);
  asm volatile("" : "+s"(seen));
  u32x4 px[3][2];
  unsigned got = 0;
#pragma unroll 1
  for (unsigned spins = 0; spins < SPIN_CAP; ++spins) {
    const unsigned now = seen & 7u & ~got;
    CFENCE();
#pragma unroll
    for (int s = 0; s < 3; ++s)
      if (now & (1u << s)) {
        const unsigned so = (unsigned)(((tile * 4 + ksp) * 3 + s) * 4 + wave) * 2048u;
#pragma unroll
        for (int n = 0; n < 2; ++n) px[s][n] = __builtin_amdgcn_raw_buffer_load_b128(rp, ln * 16 + n * 1024, so, SC1);
      }
    got |= now;
    if (got == 7u) break;
    seen = (unsigned)__ballot(__hip_atomic_load(f, RLX_AGENT) == tag);
    asm volatile("" : "+s"(seen));
  }
  unsigned pk[4][2];
#pragma unroll
  for (int n = 0; n < 4; ++n) {
    f32x4 v = c.acc[0][n];
#pragma unroll
    for (int s = 0; s < 3; ++s) {
      const unsigned lo = px[s][n >> 1][(n & 1) * 2], hi = px[s][n >> 1][(n & 1) * 2 + 1];
      v += __builtin_bit_cast(f32x4, (u32x4){lo << 16, lo & 0xFFFF0000u, hi << 16, hi & 0xFFFF0000u});
    }
    pk[n][0] = pk_bf16(v[0], v[1]);
    pk[n][1] = pk_bf16(v[2], v[3]);
  }
  const __amdgpu_buffer_rsrc_t ro = mk_rsrc(out, MT * DM * 2);
  const unsigned g = ln >> 4, row = tb * 128 + (wave >> 1) * 64 + ksp * 16 + (ln & 15);
  const unsigned vo = row * (DM * 2) + (cb * 128 + (wave & 1) * 64 + (g & 1) * 16 + (g >> 1) * 8) * 2;
#pragma unroll
  for (int h = 0; h < 2; ++h) {
    const auto s0 = __builtin_amdgcn_permlane16_swap(pk[2 * h][0], pk[2 * h + 1][0], false, false);
    const auto s1 = __builtin_amdgcn_permlane16_swap(pk[2 * h][1], pk[2 * h + 1][1], false, false);
    st16((u32x4){s0[0], s1[0], s0[1], s1[1]}, ro, vo + h * 64, 0);
  }
}

extern "C" void kernel_launch(void* const* d_in, const int* in_sizes, int n_in, void* d_out, int out_size, void* d_ws,
                              size_t ws_size, hipStream_t stream) {
  (void)hipFuncSetAttribute((const void*)ffn_fused, hipFuncAttributeMaxDynamicSharedMemorySize, LDS_BYTES);
  ffn_fused<<<256, 512, LDS_BYTES, stream>>>((const __hip_bfloat16*)d_in[0], (const __hip_bfloat16*)d_in[1],
                                             (const __hip_bfloat16*)d_in[2], (const __hip_bfloat16*)d_in[3], (char*)d_ws,
                                             (__hip_bfloat16*)d_out);
}
```

```cpp
#include <hip/hip_runtime.h>
#include <hip/hip_bf16.h>

typedef __bf16 bf16x8 __attribute__((ext_vector_type(8)));
typedef __bf16 bf16x4 __attribute__((ext_vector_type(4)));
typedef __bf16 bf16x2 __attribute__((ext_vector_type(2)));
typedef float f32x4 __attribute__((ext_vector_type(4)));
typedef float f32x2 __attribute__((ext_vector_type(2)));
typedef unsigned u32x4 __attribute__((ext_vector_type(4)));
typedef unsigned long long u64;
typedef u64 u64x4 __attribute__((ext_vector_type(4)));
typedef u64 u64x2 __attribute__((ext_vector_type(2)));
#define LAS __attribute__((address_space(3)))
#define GAS __attribute__((address_space(1)))
#define DI __device__ __forceinline__
#define SCHED() __builtin_amdgcn_sched_barrier(0)
#define CFENCE() asm volatile("" ::: "memory")
#define BAR() do { CFENCE(); __builtin_amdgcn_s_barrier(); CFENCE(); } while (0)
#define WAIT_VM(n) asm volatile("s_waitcnt vmcnt(%0)" ::"n"(n) : "memory")
#define VIS_LGKM0() __builtin_amdgcn_s_waitcnt(0xC07F)
#define VIS_VM0() __builtin_amdgcn_s_waitcnt(0x0F70)
#define RLX_AGENT __ATOMIC_RELAXED, __HIP_MEMORY_SCOPE_AGENT

constexpr int MT = 1024, DM = 1024, FF = 3584;
constexpr int KS32 = FF / 32;
constexpr unsigned ST1 = 49152;
constexpr unsigned P1B = 16384;
constexpr unsigned SL2 = 16384;
constexpr unsigned WB2 = 4 * SL2;
constexpr unsigned LDS_BYTES = 163840;
constexpr unsigned SPIN_CAP = 1u << 22;
constexpr unsigned CODE_BYTES = 15488;
constexpr int SC1 = 16;
constexpr size_t WS_H = 0;
constexpr size_t WS_PART = (size_t)MT * FF * 2;
constexpr size_t WS_FLAG = WS_PART + (size_t)64 * 4 * 3 * 8192;
constexpr size_t WS_XFLAG = WS_FLAG + 32768;
constexpr size_t WS_EPOCH = WS_XFLAG + 65536;

DI __amdgpu_buffer_rsrc_t mk_rsrc(const void* p, unsigned bytes) {
  return __builtin_amdgcn_make_buffer_rsrc((void*)p, 0, bytes, 0x00020000);
}
template <int AUX, int IMM = 0>
DI void dma16(__amdgpu_buffer_rsrc_t r, unsigned lds_off, unsigned voff, unsigned soff) {
  __builtin_amdgcn_raw_ptr_buffer_load_lds(r, (LAS void*)(size_t)lds_off, 16, voff, soff, IMM, AUX);
}
DI void touch(__amdgpu_buffer_rsrc_t r, unsigned lds_off, unsigned voff, unsigned soff) {
  __builtin_amdgcn_raw_ptr_buffer_load_lds(r, (LAS void*)(size_t)lds_off, 4, voff, soff, 0, 0);
}
DI void st16(u32x4 v, __amdgpu_buffer_rsrc_t r, unsigned voff, unsigned soff) {
  SCHED();
  __builtin_amdgcn_raw_buffer_store_b128(v, r, voff, soff, SC1);
  asm volatile("s_nop 1");
  SCHED();
}
DI bf16x8 lds_b128(unsigned addr) { return *(LAS bf16x8*)(size_t)addr; }
DI bf16x4 lds_tr(unsigned a) { return __builtin_amdgcn_ds_read_tr16_b64_v4bf16((LAS bf16x4*)(size_t)a); }
DI bf16x8 cat(bf16x4 lo, bf16x4 hi) { return __builtin_shufflevector(lo, hi, 0, 1, 2, 3, 4, 5, 6, 7); }
DI f32x4 mfma(bf16x8 a, bf16x8 b, f32x4 c) { return __builtin_amdgcn_mfma_f32_16x16x32_bf16(a, b, c, 0, 0, 0); }
DI unsigned pk_bf16(float lo, float hi) { return __builtin_bit_cast(unsigned, __builtin_convertvector((f32x2){lo, hi}, bf16x2)); }

struct P1C { bf16x8 X[8]; bf16x4 Wl[2][4], Wh[2][4]; f32x4 acc[8][4]; unsigned ax0, ax1, aw0, aw1; };

DI void p1_tile(P1C& c, int& slot) {
  unsigned delta = 0;
#pragma unroll
  for (int k = 0; k < 2; ++k) {
#pragma unroll
    for (int m = 0; m < 8; ++m) {
      if (k == 1 && m == 2) { VIS_LGKM0(); BAR(); SCHED(); }
      const unsigned ax = k ? c.ax0 : c.ax1;
      const int i = m - 2;
      const unsigned aw = ((i & 1) ? c.aw1 : c.aw0) + (i >> 1) * 16384 + (k ? 0 : 8192);
      { const int nn = (m & 1) ? 3 - 0 : 0; c.acc[m][nn] = mfma(cat(c.Wl[k][nn], c.Wh[k][nn]), c.X[m], c.acc[m][nn]); } SCHED();
      if (m >= 1 + k) { c.X[m - 1 - k] = lds_b128(ax + (m - 1 - k) * 2048); SCHED(); }
      if (k == 1 && m == 0) { c.ax1 += delta; asm volatile("" : "+v"(c.ax1)); SCHED(); }
      { const int nn = (m & 1) ? 3 - 1 : 1; c.acc[m][nn] = mfma(cat(c.Wl[k][nn], c.Wh[k][nn]), c.X[m], c.acc[m][nn]); } SCHED();
      if (m >= 2 && m < 6) { c.Wl[1 - k][i & 3] = lds_tr(aw); SCHED(); }
      if (k == 0 && m == 1) {
        asm volatile("" : "+s"(slot));
        delta = slot == 2 ? (unsigned)(-2 * (int)ST1) : ST1;
        asm volatile("" : "+s"(delta));
        SCHED();
      }
      if (k == 0 && m == 6) { c.ax0 += delta; asm volatile("" : "+v"(c.ax0)); SCHED(); }
      if (k == 0 && m == 7) { c.aw1 += delta; asm volatile("" : "+v"(c.aw1)); SCHED(); }
      { const int nn = (m & 1) ? 3 - 2 : 2; c.acc[m][nn] = mfma(cat(c.Wl[k][nn], c.Wh[k][nn]), c.X[m], c.acc[m][nn]); } SCHED();
      if (m >= 2 && m < 6) { c.Wh[1 - k][i & 3] = lds_tr(aw + 1024); SCHED(); }
      if (m == 7 && k == 1) { c.X[6] = lds_b128(ax + 6 * 2048); SCHED(); }
      if (k == 0 && m == 1) { slot = slot == 2 ? 0 : slot + 1; asm volatile("" : "+s"(slot)); SCHED(); }
      if (k == 0 && m == 6) { c.aw0 += delta; asm volatile("" : "+v"(c.aw0)); SCHED(); }
      { const int nn = (m & 1) ? 3 - 3 : 3; c.acc[m][nn] = mfma(cat(c.Wl[k][nn], c.Wh[k][nn]), c.X[m], c.acc[m][nn]); } SCHED();
      if (m == 7) { c.X[7] = lds_b128(ax + 7 * 2048); SCHED(); }
    }
  }
}

struct P2C { bf16x8 H[2][4]; bf16x4 Wl[2][4], Wh[2][4]; f32x4 acc[4][4]; unsigned ah, aw[4]; };

template <int B>
DI void p2_read(P2C& c, int e, unsigned ks) {
  if (e == 2 || e >= 9) { const int m = e == 2 ? 0 : e - 8; c.H[B][m] = lds_b128(c.ah + m * 2048 + ks * 1024); }
  else {
    const int f = e < 2 ? e : e - 1, n = f >> 1;
    if (f & 1) c.Wh[B][n] = lds_tr(c.aw[n] + ks * 8192 + 1024); else c.Wl[B][n] = lds_tr(c.aw[n] + ks * 8192);
  }
}
template <int HF, bool LAST = false>
DI void p1_half(P1C& c, int& slot, unsigned flip, P2C* c2 = nullptr) {
  unsigned delta = 0;
#pragma unroll
  for (int gp = 0; gp < 16; ++gp) {
    const int m = gp >> 1, f = ((gp ^ m) & 1) * 2;
    c.acc[m][HF + f] = mfma(cat(c.Wl[0][f], c.Wh[0][f]), c.X[m], c.acc[m][HF + f]); SCHED();
    if (gp == 0) { asm volatile("" : "+s"(slot)); delta = slot == 2 ? (unsigned)(-2 * (int)ST1) : ST1; asm volatile("" : "+s"(delta)); }
    if (gp >= 2 && !(gp & 1)) c.X[m - 1] = lds_b128(c.ax1 + (m - 1) * 2048);
    if (gp == 1) c.Wl[1][0] = lds_tr(c.aw0 + 8192);
    if (gp == 3) c.Wh[1][0] = lds_tr(c.aw0 + 8192 + 1024);
    if (gp == 5) c.Wl[1][2] = lds_tr(c.aw0 + 16384 + 8192);
    if (gp == 7) c.Wh[1][2] = lds_tr(c.aw0 + 16384 + 8192 + 1024);
    if (gp == 9) { c.aw0 = (c.aw0 + delta) ^ flip; asm volatile("" : "+v"(c.aw0)); }
    if (gp == 11) { c.ax0 += delta; asm volatile("" : "+v"(c.ax0)); }
    if (gp == 13) { slot = slot == 2 ? 0 : slot + 1; asm volatile("" : "+s"(slot)); }
    if (gp == 15) c.X[7] = lds_b128(c.ax1 + 7 * 2048);
    SCHED();
  }
#pragma unroll
  for (int gp = 0; gp < 16; ++gp) {
    const int m = gp >> 1, f = ((gp ^ m) & 1) * 2;
    if (gp == 6) { VIS_LGKM0(); BAR(); SCHED(); }
    c.acc[m][HF + f] = mfma(cat(c.Wl[1][f], c.Wh[1][f]), c.X[m], c.acc[m][HF + f]); SCHED();
    if (LAST) {
      if (gp >= 6) p2_read<0>(*c2, gp - 6, 0);
      if (gp >= 14) p2_read<0>(*c2, gp - 4, 0);
      SCHED();
      continue;
    }
    if (gp == 0) { c.ax1 += delta; asm volatile("" : "+v"(c.ax1)); }
    if (gp == 6) { c.Wl[0][0] = lds_tr(c.aw0); c.Wh[0][0] = lds_tr(c.aw0 + 1024); }
    if (gp == 7) c.X[0] = lds_b128(c.ax0);
    if (gp == 8) c.Wl[0][2] = lds_tr(c.aw0 + 16384);
    if (gp == 9) c.Wh[0][2] = lds_tr(c.aw0 + 16384 + 1024);
    if (gp >= 10) c.X[gp - 9] = lds_b128(c.ax0 + (gp - 9) * 2048);
    if (gp == 15) c.X[7] = lds_b128(c.ax0 + 7 * 2048);
    SCHED();
  }
}

template <int HF>
DI void p1_epi(P1C& c, unsigned la0, unsigned la2, __amdgpu_buffer_rsrc_t rh, unsigned vo0) {
  u32x4 o[4];
  f32x4 ta = c.acc[0][HF] * -1.44269504088896f, tb = c.acc[1][HF] * -1.44269504088896f;
  SCHED();
#pragma unroll
  for (int m = 0; m < 8; m += 2) {
#pragma unroll
    for (int i = 0; i < 4; ++i) ta[i] = __builtin_amdgcn_exp2f(ta[i]);
#pragma unroll
    for (int i = 0; i < 4; ++i) tb[i] = __builtin_amdgcn_exp2f(tb[i]);
    SCHED();
    f32x4 da = ta + 1.0f, db = tb + 1.0f;
    SCHED();
#pragma unroll
    for (int i = 0; i < 4; ++i) da[i] = __builtin_amdgcn_rcpf(da[i]);
#pragma unroll
    for (int i = 0; i < 4; ++i) db[i] = __builtin_amdgcn_rcpf(db[i]);
    SCHED();
    if (m < 6) { ta = c.acc[m + 2][HF] * -1.44269504088896f; tb = c.acc[m + 3][HF] * -1.44269504088896f; }
    unsigned vo = vo0 + m * (KS32 * 1024);
    asm volatile("" : "+v"(vo));
    SCHED();
    da *= c.acc[m][HF]; db *= c.acc[m + 1][HF];
    SCHED();
    da *= c.acc[m][HF + 2]; db *= c.acc[m + 1][HF + 2];
    SCHED();
    const auto s0 = __builtin_amdgcn_permlane16_swap(pk_bf16(da[0], da[1]), pk_bf16(db[0], db[1]), false, false);
    const auto s1 = __builtin_amdgcn_permlane16_swap(pk_bf16(da[2], da[3]), pk_bf16(db[2], db[3]), false, false);
    o[m >> 1] = (u32x4){s0[0], s1[0], s0[1], s1[1]};
    SCHED();
    __builtin_amdgcn_raw_buffer_store_b128(o[m >> 1], rh, vo, 0, SC1);
    *(LAS u32x4*)(size_t)(((m & 2) ? la2 : la0) + (m >> 2) * 8192) = o[m >> 1];
    SCHED();
  }
#pragma unroll
  for (int m = 0; m < 4; ++m) asm volatile("" ::"v"(o[m]));
}

template <bool FIRST>
DI void p2_tile(P2C& c, int& sw6, int& sh4) {
  unsigned dw = 0, dh = 0;
#pragma unroll
  for (int gp = 0; gp < 16; ++gp) {
    const int m = gp >> 2, n = (m & 1) ? 3 - (gp & 3) : (gp & 3);
    c.acc[m][n] = mfma(cat(c.Wl[0][n], c.Wh[0][n]), c.H[0][m], FIRST ? (f32x4){0.f, 0.f, 0.f, 0.f} : c.acc[m][n]); SCHED();
    if (gp < 12) { p2_read<1>(c, gp, 1); SCHED(); }
    if (gp == 12) { asm volatile("" : "+s"(sw6)); dw = sw6 == 5 ? (unsigned)(-5 * (int)SL2) : SL2; asm volatile("" : "+s"(dw)); SCHED(); }
    if (gp == 13) { asm volatile("" : "+s"(sh4)); dh = sh4 == 3 ? (unsigned)(-3 * (int)SL2) : SL2; asm volatile("" : "+s"(dh)); SCHED(); }
    if (gp == 14) { c.aw[0] += dw; c.aw[1] += dw; asm volatile("" : "+v"(c.aw[0]), "+v"(c.aw[1])); SCHED(); }
    if (gp == 15) { c.aw[2] += dw; c.aw[3] += dw; asm volatile("" : "+v"(c.aw[2]), "+v"(c.aw[3])); SCHED(); }
  }
#pragma unroll
  for (int gp = 0; gp < 16; ++gp) {
    const int m = gp >> 2, n = (m & 1) ? 3 - (gp & 3) : (gp & 3);
    if (gp == 4) { VIS_LGKM0(); BAR(); SCHED(); }
    c.acc[m][n] = mfma(cat(c.Wl[1][n], c.Wh[1][n]), c.H[1][m], c.acc[m][n]); SCHED();
    if (gp == 0) { c.ah += dh; asm volatile("" : "+v"(c.ah)); SCHED(); }
    if (gp == 1) { sw6 = sw6 == 5 ? 0 : sw6 + 1; asm volatile("" : "+s"(sw6)); SCHED(); }
    if (gp == 2) { sh4 = (sh4 + 1) & 3; asm volatile("" : "+s"(sh4)); SCHED(); }
    if (gp >= 4) { p2_read<0>(c, gp - 4, 0); SCHED(); }
  }
}

template <int B>
DI void p2_fread(P2C& c, int e, unsigned ks) {
  if (e == 2 || e >= 9) { const int m = e == 2 ? 1 : e - 7; c.H[B][m] = lds_b128(c.ah + m * 2048 + ks * 1024); }
  else p2_read<B>(c, e, ks);
}
DI void p2_ftile(P2C& c, int& sw6, int& sh4) {
  unsigned dw = 0, dh = 0;
#pragma unroll
  for (int gp = 0; gp < 12; ++gp) {
    const int m = 1 + (gp >> 2), n = (m & 1) ? (gp & 3) : 3 - (gp & 3);
    c.acc[m][n] = mfma(cat(c.Wl[0][n], c.Wh[0][n]), c.H[0][m], c.acc[m][n]); SCHED();
    if (gp < 11) p2_fread<1>(c, gp, 1);
    if (gp == 5) { asm volatile("" : "+s"(sw6)); dw = sw6 == 5 ? (unsigned)(-5 * (int)SL2) : SL2; asm volatile("" : "+s"(dw)); }
    if (gp == 6) { asm volatile("" : "+s"(sh4)); dh = sh4 == 3 ? (unsigned)(-3 * (int)SL2) : SL2; asm volatile("" : "+s"(dh)); }
    if (gp == 7) { c.aw[0] += dw; asm volatile("" : "+v"(c.aw[0])); }
    if (gp == 9) { c.aw[1] += dw; asm volatile("" : "+v"(c.aw[1])); }
    if (gp == 10) { c.aw[2] += dw; asm volatile("" : "+v"(c.aw[2])); }
    if (gp == 11) { c.aw[3] += dw; c.ah += dh; asm volatile("" : "+v"(c.aw[3]), "+v"(c.ah)); }
    SCHED();
  }
  BAR();
  SCHED();
#pragma unroll
  for (int gp = 0; gp < 12; ++gp) {
    const int m = 1 + (gp >> 2), n = (m & 1) ? (gp & 3) : 3 - (gp & 3);
    c.acc[m][n] = mfma(cat(c.Wl[1][n], c.Wh[1][n]), c.H[1][m], c.acc[m][n]); SCHED();
    if (gp < 11) p2_fread<0>(c, gp, 0);
    if (gp == 11) { sw6 = sw6 == 5 ? 0 : sw6 + 1; sh4 = (sh4 + 1) & 3; asm volatile("" : "+s"(sw6), "+s"(sh4)); }
    SCHED();
  }
}
DI void p2_otile(P2C& c, unsigned dw, unsigned dh) {
#pragma unroll
  for (int e = 0; e < 9; ++e) p2_read<0>(c, e, 0);
#pragma unroll
  for (int e = 0; e < 9; ++e) p2_read<1>(c, e, 1);
#pragma unroll
  for (int k = 0; k < 2; ++k)
#pragma unroll
    for (int n = 0; n < 4; ++n) c.acc[0][n] = mfma(cat(c.Wl[k][n], c.Wh[k][n]), c.H[k][0], c.acc[0][n]);
  c.ah += dh;
#pragma unroll
  for (int n = 0; n < 4; ++n) c.aw[n] += dw;
}

__global__ __launch_bounds__(512, 2) void ffn_fused(const __hip_bfloat16*, const __hip_bfloat16*, const __hip_bfloat16*,
                                                    const __hip_bfloat16*, char*, __hip_bfloat16*) {
  extern __shared__ __attribute__((aligned(16))) char smem[];
  u64x4 ka; u64x2 kb;
  asm volatile("s_load_dwordx8 %0, %2, 0x0\n\ts_load_dwordx4 %1, %2, 0x20" : "=&s"(ka), "=&s"(kb) : "s"(__builtin_amdgcn_kernarg_segment_ptr()));
  u64 pc0; asm volatile("s_getpc_b64 %0" : "=s"(pc0));
  const int bid = blockIdx.x, xcd = bid & 7, idx = bid >> 3;
  const int ksp = xcd >> 1, xe = xcd & 1, tb = idx >> 2, u = idx & 3;
  const bool spare = u == 3 && (tb >> 2) != xe;
  const int j = u < 3 ? xe * 4 + u : 3;
  const int fb = ksp * 7 + j, cb = xe * 4 + u;
  const int wave = __builtin_amdgcn_readfirstlane(threadIdx.x >> 6), l = threadIdx.x & 63;
  const unsigned L0 = (unsigned)(size_t)(LAS char*)smem;
  if (wave == 3) touch(mk_rsrc((const void*)(pc0 & ~(u64)127), CODE_BYTES), L0 + 8192 + 768, (idx + (l >> 4) * 32) * 128, 0);
  unsigned vx0, vw0, vp0;
  {
    const int xr = 8 * wave + (l >> 3);
    const unsigned sx = ((xr >> 1) & 3) | ((((xr >> 2) ^ (xr >> 3)) & 1) << 2);
    vx0 = xr * 2048 + (((l & 7) ^ sx) << 4);
    const unsigned sw = (l >> 4) | ((wave & 2) << 1);
    vw0 = (4 * wave + (l >> 4)) * (FF * 2) + (((((l & 15) >> 1) ^ sw)) << 5) + ((l & 1) << 4);
    vp0 = wave ? (idx * 32 + (l >> 1)) * 2048 + (l & 1) * 128
               : ((idx & 15) * 8 + (l >> 3)) * (FF * 2) + (ksp * 7 + xe * 3) * 256 + (l & 7) * 128;
  }
  if (spare) {
    vw0 = ((((tb & 1) * 4 + (wave & 3)) * 8 + (l >> 3)) + (2 + (wave >> 2)) * 64) * (FF * 2) + (ksp * 7 + xe * 3) * 256 + (l & 7) * 128;
    vx0 = (xcd * 128 + (tb & 3) * 32 + (l & 31)) * 2048 + ((wave - 1) * 2 + (l >> 5)) * 128;
  }
  asm volatile("s_waitcnt lgkmcnt(0)" : "+s"(ka), "+s"(kb), "+v"(vx0), "+v"(vw0), "+v"(vp0));
  const void *x = (const void*)ka.x, *wg = (const void*)ka.y, *wu = (const void*)ka.z, *wd = (const void*)ka.w;
  char* ws = (char*)kb.x;
  void* out = (void*)kb.y;
  u64 tag = __hip_atomic_load((GAS u64*)(ws + WS_EPOCH), RLX_AGENT);
  CFENCE();
  GAS u64* hflag = (GAS u64*)(ws + WS_FLAG) + (tb * 4 + ksp) * 128;
  auto unit_table = [&]() {
    const u64 a = (0xCA86420CA86420ull >> (4 * j)) & 0xFFFFFFFull, b = a | 0x1111111ull;
    return spare ? (a | (b << 28)) : ((a & 15ull) | ((b & 15ull) << 4) | ((a >> 4) << 8) | ((b >> 4) << 32));
  };
  const __amdgpu_buffer_rsrc_t rx = mk_rsrc(x, MT * DM * 2), rg = mk_rsrc(wg, DM * FF * 2), ru = mk_rsrc(wu, DM * FF * 2);
  if (wave < 2) touch(wave ? rx : ((idx & 16) ? ru : rg), L0 + 8192 + wave * 256, vp0, 0);
  if (spare) {
    if (wave == 2 || wave == 3) touch(rx, L0 + wave * 256, vx0, 0);
    touch((tb & 2) ? ru : rg, L0 + wave * 256, vw0, 0);
  }
  CFENCE();
  if (__builtin_expect(!spare, 1)) {
    const unsigned sx0 = tb * 128 * 2048, sw0 = fb * 256, lb = L0 + P1B + wave * 1024;
#pragma unroll
    for (int a = 0; a < 2; ++a) {
      dma16<0>(rg, lb + 16384 + a * 8192, vw0, sw0 + a * 32 * (FF * 2));
      dma16<0>(ru, lb + 32768 + a * 8192, vw0, sw0 + a * 32 * (FF * 2));
      dma16<0>(rx, lb + a * 8192, vx0, sx0 + a * 64 * 2048);
    }
    if (wave >= 4) {
#pragma unroll
      for (int a = 0; a < 2; ++a) {
        dma16<0>(rg, lb + ST1 + 16384 + (a - 1) * 4096, vw0, sw0 + (64 + (a - 1) * 16) * (FF * 2));
        dma16<0>(ru, lb + ST1 + 32768 + (a - 1) * 4096, vw0, sw0 + (64 + (a - 1) * 16) * (FF * 2));
        dma16<0>(rx, lb + ST1 + (a - 1) * 4096, vx0 - 32 * 2048, sx0 + a * 32 * 2048 + 128);
      }
    }
  } else {
    const u64 t_start = __builtin_amdgcn_s_memrealtime();
    const int part = (tb & 3) * 4 + (wave & 3);
    const __amdgpu_buffer_rsrc_t rs = (part & 8) ? ru : rg;
    const unsigned vo = ((part & 7) * 8 + (l >> 3)) * (FF * 2) + (ksp * 7 + xe * 3) * 256 + (l & 7) * 128;
#pragma unroll 1
    for (int t = 4 + (wave >> 2), it = 3; t < 16; t += 2, ++it) {
      touch(rs, L0 + wave * 256, vo, t * 64 * (FF * 2));
      if (it == wave)
        touch(rx, L0 + wave * 256, (xcd * 128 + (tb & 3) * 32 + (l & 31)) * 2048 + (wave * 2 + (l >> 5)) * 128, 0);
      WAIT_VM(1);
    }
    if (wave < 4 || wave >= 6) {
      const __amdgpu_buffer_rsrc_t rw = mk_rsrc(wd, FF * DM * 2);
      const unsigned vw = (ksp * 896 + (l >> 3)) * 2048 + xe * 1024 + (l & 7) * 128;
#pragma unroll 1
      for (int e = (tb & 3) * 6 + (wave < 4 ? wave : wave - 2); e < 112; e += 24) {
        if (__builtin_amdgcn_s_memrealtime() - t_start > 1000) break;
        const int un = e >> 3, r = (un < 7 ? 2 * un : 2 * un - 13) * 8 + (e & 7);
        touch(rw, L0 + wave * 256, vw, r * 8 * 2048);
        WAIT_VM(1);
      }
    }
    VIS_VM0();
    CFENCE();
  }

  if (wave >= 6) {
    const int lw = wave - 6;
    if (!spare) { WAIT_VM(6); BAR(); }
    asm volatile("" : "+v"(tag));
    tag += 1ull;
    asm volatile("" : "+v"(tag));
    WAIT_VM(0);
    unsigned w0 = 0, w1 = 0;
    const u64 tbl = unit_table();
    GAS u64* fw;
    { const unsigned un = (unsigned)(tbl >> (4 * ((l >> 2) < 14 ? (l >> 2) : 13))) & 15u; fw = hflag + (un >> 1) * 16 + (un & 1) * 4 + (l & 3); }
    if (!spare) {
#pragma unroll 1
      for (int t = 1; t < 15; ++t) BAR();
      const __amdgpu_buffer_rsrc_t rw = mk_rsrc(wd, FF * DM * 2);
      const int me = idx - ((xe ? (tb < 4 ? tb : 4) : (tb > 4 ? tb - 4 : 0)));
      const unsigned vb = (ksp * 896 + me * 32 + lw * 16 + (l >> 3)) * 2048 + xe * 1024 + (l & 7) * 128;
      w0 = __builtin_amdgcn_raw_buffer_load_b32(rw, vb, 0, 0);
      w1 = __builtin_amdgcn_raw_buffer_load_b32(rw, vb, 8 * 2048, 0);
      BAR();
    }
    BAR();
    const __amdgpu_buffer_rsrc_t rh = mk_rsrc(ws + WS_H, MT * FF * 2);
    unsigned tofs[4];
#pragma unroll
    for (int a = 0; a < 4; ++a) tofs[a] = (unsigned)((tb * 8 + lw * 4 + ((a + ksp) & 3)) * KS32 + ksp * 28) * 1024u;
    auto issue_h = [&](int kt) {
      const unsigned sb = ((unsigned)(tbl >> (4 * kt)) & 15u) * 2048u;
      const unsigned lb = L0 + (kt & 3) * SL2 + lw * 8192;
#pragma unroll
      for (int a = 0; a < 4; ++a) {
        dma16<SC1>(rh, lb + a * 2048, l * 16, sb + tofs[a]);
        dma16<SC1, 1024>(rh, lb + a * 2048, l * 16, sb + tofs[a]);
      }
    };
    int nrdy = 0;
    auto look = [&](u64 v) { nrdy = __builtin_ctzll(~(__ballot(v == tag) | (spare ? 0ull : 255ull)) | (1ull << 63)) >> 2; CFENCE(); };
    if (spare) {
      if (l == 0) __hip_atomic_store(hflag + 7 * 16 + lw, tag, RLX_AGENT);
#pragma unroll 1
      for (unsigned spins = 0; spins < SPIN_CAP && nrdy < 1; ++spins) { __builtin_amdgcn_s_sleep(8); look(__hip_atomic_load(fw, RLX_AGENT)); }
    } else {
      BAR(); BAR(); BAR();
#pragma unroll 1
      for (int i = 0; i < 3 && nrdy < 4; ++i) look(__hip_atomic_load(fw, RLX_AGENT));
    }
    VIS_VM0();
    asm volatile("" ::"v"(w0), "v"(w1));
    int nx = spare ? 0 : 2;
#pragma unroll 1
    for (int kt = spare ? 0 : 1; kt < 14; ++kt) {
      if (nx == kt + 2) {
        const int fe = nrdy - 3;
#pragma unroll 1
        for (; kt < fe; ++kt) { issue_h(kt + 2); WAIT_VM(16); BAR(); }
        nx = kt + 2;
      }
      const int lim = kt < 11 ? (kt ? kt + 2 : 3) : 13;
      if (__builtin_expect(kt >= nrdy, 0)) {
#pragma unroll 1
        for (unsigned spins = 0; spins < SPIN_CAP && kt >= nrdy; ++spins) look(__hip_atomic_load(fw, RLX_AGENT));
      }
      u64 pv;
      const bool pend = lim + 1 >= nrdy;
      if (__builtin_expect(pend, 0)) asm volatile("global_load_dwordx2 %0, %1, off sc1" : "=v"(pv) : "v"(fw) : "memory");
      const int n0 = nx;
#pragma unroll 1
      while (nx <= lim && nx < nrdy) { issue_h(nx); ++nx; }
      if (__builtin_expect(nx >= kt + 3, 1)) WAIT_VM(16); else if (nx == kt + 2) WAIT_VM(8); else WAIT_VM(0);
      BAR();
      if (__builtin_expect(pend, 0)) {
        if (nx > n0) WAIT_VM(8); else WAIT_VM(0);
        asm volatile("" : "+v"(pv));
        look(pv);
      }
    }
    BAR();
    if (bid == 0 && lw == 0) {
      GAS u64* fa = (GAS u64*)(ws + WS_FLAG);
#pragma unroll 1
      for (unsigned spins = 0; spins < SPIN_CAP; ++spins) {
        bool ok = true;
#pragma unroll
        for (int i = 0; i < 4; ++i) ok &= __hip_atomic_load(fa + (i * 64 + l) * 16, RLX_AGENT) == tag;
        if (__all(ok)) break;
        __builtin_amdgcn_s_sleep(2);
      }
      if (l == 0) __hip_atomic_store((GAS u64*)(ws + WS_EPOCH), tag, RLX_AGENT);
    }
    return;
  }
  if (wave >= 4) {
    const int lw = wave - 4;
    const __amdgpu_buffer_rsrc_t rw = mk_rsrc(wd, FF * DM * 2);
    const unsigned vd = (8 * lw + (l >> 4)) * (DM * 2) + (((((l & 15) >> 1) ^ ((l >> 4) | (lw << 2)))) << 5) + ((l & 1) << 4);
    const unsigned sd0 = cb * 256 + ksp * 896 * (DM * 2);
    const unsigned vd2 = vd + 4 * (DM * 2) - 1024;
    u64 tbl = 0;
    auto issue_w = [&](int kt, unsigned slot) {
      const unsigned sb = sd0 + ((unsigned)(tbl >> (4 * kt)) & 15u) * (64 * DM * 2);
      const unsigned lb = L0 + WB2 + slot * SL2 + lw * 2048;
#pragma unroll
      for (int a = 0; a < 4; ++a) {
        dma16<0>(rw, lb + a * 4096, vd, sb + a * 16 * (DM * 2));
        dma16<0, 1024>(rw, lb + a * 4096, vd2, sb + a * 16 * (DM * 2));
      }
    };
    if (!spare) {
      const int xr = 8 * lw + (l >> 3);
      const unsigned sx = ((xr >> 1) & 3) | ((((xr >> 2) ^ (xr >> 3)) & 1) << 2);
      const unsigned vx = xr * 2048 + (((l & 7) ^ sx) << 4);
      const unsigned sw = (l >> 4) | (lw << 2);
      const unsigned vw = (8 * lw + (l >> 4)) * (FF * 2) + (((((l & 15) >> 1) ^ sw)) << 5) + ((l & 1) << 4);
      const unsigned vw2 = vw + 4 * (FF * 2) - 1024, vx2 = vx + 32768 - 2048;
      unsigned lb = L0 + P1B + ST1 + 8192, so = fb * 256 + 96 * (FF * 2), sxo = tb * 128 * 2048 + 128 + 131072;
      int n = 3;
#pragma unroll 1
      for (int t = 0; t < 14; ++t) {
        if (t == 0) WAIT_VM(6); else if (t == 1) WAIT_VM(12); else WAIT_VM(24);
        BAR();
        const int lim = t ? 2 * t + 6 : 5;
#pragma unroll 1
        for (; n < lim; ++n) {
#pragma unroll
          for (int a = 0; a < 2; ++a) {
            dma16<0>(rg, lb + 16384 + a * 4096 + lw * 2048, vw, so + a * 16 * (FF * 2));
            dma16<0, 1024>(rg, lb + 16384 + a * 4096 + lw * 2048, vw2, so + a * 16 * (FF * 2));
            dma16<0>(ru, lb + 32768 + a * 4096 + lw * 2048, vw, so + a * 16 * (FF * 2));
            dma16<0, 1024>(ru, lb + 32768 + a * 4096 + lw * 2048, vw2, so + a * 16 * (FF * 2));
            dma16<0>(rx, lb + a * 4096 + lw * 1024, vx, sxo + a * 65536);
            dma16<0, 2048>(rx, lb + a * 4096 + lw * 1024, vx2, sxo + a * 65536);
          }
          so += 32 * (FF * 2);
          if (n & 1) { lb += ST1 - 8192; if (lb == L0 + P1B + 3 * ST1) lb = L0 + P1B; sxo += 128 - 131072; }
          else { lb += 8192; sxo += 131072; }
        }
      }
      WAIT_VM(24); BAR();
      WAIT_VM(0); BAR();
      tbl = unit_table();
      BAR();
      BAR();
      issue_w(0, 0);
      BAR();
      issue_w(1, 1);
      WAIT_VM(8); BAR();
#pragma unroll 1
      for (int kt = 2; kt < 4; ++kt) issue_w(kt, kt);
      WAIT_VM(16); BAR();
#pragma unroll 1
      for (int kt = 4; kt < 6; ++kt) issue_w(kt, kt);
      WAIT_VM(24); BAR();
      issue_w(6, 0);
    } else {
      tbl = unit_table();
#pragma unroll 1
      for (int kt = 0; kt < 6; ++kt) issue_w(kt, kt);
      BAR();
      WAIT_VM(0); BAR();
      BAR();
      BAR();
      issue_w(6, 0);
    }
    unsigned slot = 1;
#pragma unroll 1
    for (int kt = 3; kt < 10; ++kt) {
      WAIT_VM(24); BAR();
      issue_w(kt + 4, slot);
      slot = slot == 5 ? 0 : slot + 1;
    }
    WAIT_VM(24); BAR();
    WAIT_VM(16); BAR();
    WAIT_VM(8); BAR();
    WAIT_VM(0); BAR();
    BAR();
    return;
  }

  P1C c1;
  P2C c;
  unsigned la0, la2, vo0;
  {
    const int g = l >> 4, q = (l & 15) >> 2, p = l & 3, r15 = l & 15;
    const unsigned b = L0 + P1B;
    const unsigned sx = ((r15 >> 1) & 3) | ((((r15 >> 2) ^ (r15 >> 3)) & 1) << 2);
    c1.ax0 = b + r15 * 128 + ((g ^ sx) << 4);
    c1.ax1 = b + r15 * 128 + (((4 + g) ^ sx) << 4);
    const unsigned sw = q | ((g & 1) << 2);
    c1.aw0 = b + 16384 + (8 * g + q) * 256 + p * 8 + ((wave ^ sw) << 5);
    c1.aw1 = b + 16384 + (8 * g + q) * 256 + p * 8 + (((4 + wave) ^ sw) << 5);
    asm volatile("" : "+v"(c1.ax0), "+v"(c1.ax1), "+v"(c1.aw0), "+v"(c1.aw1));
    c.ah = L0 + (wave >> 1) * 8192 + l * 16;
#pragma unroll
    for (int n = 0; n < 4; ++n) c.aw[n] = L0 + WB2 + (8 * g + q) * 256 + p * 8 + (((4 * (wave & 1) + n) ^ sw) << 5);
    const unsigned g1 = g & 1, in = ((wave & 1) * 2 + (g >> 1)) * 256 + r15 * 16 + (wave >> 1) * 1024;
    la0 = L0 + ((g1 - ksp) & 3) * 2048 + in; la2 = L0 + ((g1 + 2 - ksp) & 3) * 2048 + in;
    vo0 = (unsigned)((tb * 8 + g1) * KS32 + fb * 4 + 2) * 1024u + in;
    asm volatile("" : "+v"(c.ah), "+v"(c.aw[0]), "+v"(c.aw[1]), "+v"(c.aw[2]), "+v"(c.aw[3]), "+v"(la0), "+v"(la2), "+v"(vo0));
  }
#pragma unroll
  for (int m = 0; m < 8; ++m)
#pragma unroll
    for (int n = 0; n < 4; ++n) { c1.acc[m][n] = (f32x4){0.f, 0.f, 0.f, 0.f}; asm volatile("" : "+v"(c1.acc[m][n])); }
  SCHED();
  asm volatile("" : "+v"(tag));
  tag += 1ull;
  tag = ((u64)(unsigned)__builtin_amdgcn_readfirstlane((unsigned)(tag >> 32)) << 32) | (unsigned)__builtin_amdgcn_readfirstlane((unsigned)tag);
  asm volatile("" : "+s"(tag));
  __builtin_amdgcn_s_waitcnt(0x0070);
  SCHED();
  if (!spare) {
    BAR();
#pragma unroll
    for (int i = 0; i < 4; ++i) {
      const unsigned a = ((i & 1) ? c1.aw1 : c1.aw0) + (i >> 1) * 16384;
      c1.Wl[0][i] = lds_tr(a); c1.Wh[0][i] = lds_tr(a + 1024);
      if (i == 0) c1.X[0] = lds_b128(c1.ax0);
    }
#pragma unroll
    for (int m = 1; m < 8; ++m) c1.X[m] = lds_b128(c1.ax0 + m * 2048);
    SCHED();
    int slot = 0;
#pragma unroll 1
    for (int t = 0; t < 13; ++t) p1_tile(c1, slot);
#pragma unroll 1
    for (int t = 0; t < 3; ++t) p1_half<0>(c1, slot, t == 2 ? 128u : 0u);
    const __amdgpu_buffer_rsrc_t rh = mk_rsrc(ws + WS_H, MT * FF * 2);
    p1_epi<0>(c1, la0, la2, rh, vo0 - 2048);
#pragma unroll 1
    for (int t = 0; t < 2; ++t) {
      p1_half<1>(c1, slot, 0u);
      if (t == 0) { WAIT_VM(0); if (l == 0) __hip_atomic_store(hflag + j * 16 + wave, tag, RLX_AGENT); }
    }
    p1_half<1, true>(c1, slot, 0u, &c);
    p1_epi<1>(c1, la0 + SL2, la2 + SL2, rh, vo0);
  } else {
    BAR();
    BAR();
#pragma unroll
    for (int e = 0; e < 12; ++e) p2_read<0>(c, e, 0);
    SCHED();
  }

  int tz = threadIdx.x; asm volatile("" : "+v"(tz));
  const int ln = tz & 63;
  int sw6 = 0, sh4 = 0;
  p2_tile<true>(c, sw6, sh4);
  if (!spare) {
    WAIT_VM(0);
    if (ln == 0) __hip_atomic_store(hflag + j * 16 + 4 + wave, tag, RLX_AGENT);
  }
#pragma unroll 1
  for (int kt = 1; kt < 10; ++kt) p2_tile<false>(c, sw6, sh4);
#pragma unroll 1
  for (int kt = 10; kt < 14; ++kt) p2_ftile(c, sw6, sh4);
  const int tile = tb * 8 + cb;
  const __amdgpu_buffer_rsrc_t rp = mk_rsrc(ws + WS_PART, 64u * 4 * 3 * 8192);
#pragma unroll
  for (int mm = 1; mm < 4; ++mm) {
    const unsigned so = (unsigned)(((tile * 4 + ((ksp + mm) & 3)) * 3 + (mm - 1)) * 4 + wave) * 2048u;
#pragma unroll
    for (int n = 0; n < 4; n += 2) {
      const f32x4 a = c.acc[mm][n], b = c.acc[mm][n + 1];
      st16((u32x4){pk_bf16(a[0], a[1]), pk_bf16(a[2], a[3]), pk_bf16(b[0], b[1]), pk_bf16(b[2], b[3])}, rp, ln * 16 + n * 512, so);
    }
  }
#pragma unroll
  for (int n = 0; n < 4; ++n) c.aw[n] += 2 * SL2;
#pragma unroll 1
  for (int kt = 10; kt < 14; ++kt) p2_otile(c, kt == 11 ? (unsigned)(-5 * (int)SL2) : SL2, kt == 11 ? (unsigned)(-3 * (int)SL2) : SL2);
  SCHED();
  GAS u64* xf = (GAS u64*)(ws + WS_XFLAG);
  GAS u64* f = xf + ((tile * 4 + ksp) * 4 + wave) * 8 + (ln < 3 ? ln : 0);
  u64 fl = __hip_atomic_load(f, RLX_AGENT);
  WAIT_VM(1);
  {
    typedef unsigned u32x2 __attribute__((ext_vector_type(2)));
    const unsigned fo = ln < 3 ? (unsigned)(((tile * 4 + ((ksp + ln + 1) & 3)) * 4 + wave) * 8 + ln) * 8u : 0x80000000u;
    __builtin_amdgcn_raw_buffer_store_b64((u32x2){(unsigned)tag, (unsigned)(tag >> 32)}, mk_rsrc(ws + WS_XFLAG, 65536), fo, 0, SC1);
  }
  SCHED();
  asm volatile("" : "+v"(fl));
  unsigned seen = (unsigned)__ballot(fl == tag);
  asm volatile("" : "+s"(seen));
  u32x4 px[3][2];
  unsigned got = 0;
#pragma unroll 1
  for (unsigned spins = 0; spins < SPIN_CAP; ++spins) {
    const unsigned now = seen & 7u & ~got;
    CFENCE();
#pragma unroll
    for (int s = 0; s < 3; ++s)
      if (now & (1u << s)) {
        const unsigned so = (unsigned)(((tile * 4 + ksp) * 3 + s) * 4 + wave) * 2048u;
#pragma unroll
        for (int n = 0; n < 2; ++n) px[s][n] = __builtin_amdgcn_raw_buffer_load_b128(rp, ln * 16 + n * 1024, so, SC1);
      }
    got |= now;
    if (got == 7u) break;
    seen = (unsigned)__ballot(__hip_atomic_load(f, RLX_AGENT) == tag);
    asm volatile("" : "+s"(seen));
  }
  unsigned pk[4][2];
#pragma unroll
  for (int n = 0; n < 4; ++n) {
    f32x4 v = c.acc[0][n];
#pragma unroll
    for (int s = 0; s < 3; ++s) {
      const unsigned lo = px[s][n >> 1][(n & 1) * 2], hi = px[s][n >> 1][(n & 1) * 2 + 1];
      v += __builtin_bit_cast(f32x4, (u32x4){lo << 16, lo & 0xFFFF0000u, hi << 16, hi & 0xFFFF0000u});
    }
    pk[n][0] = pk_bf16(v[0], v[1]);
    pk[n][1] = pk_bf16(v[2], v[3]);
  }
  const __amdgpu_buffer_rsrc_t ro = mk_rsrc(out, MT * DM * 2);
  const unsigned g = ln >> 4, row = tb * 128 + (wave >> 1) * 64 + ksp * 16 + (ln & 15);
  const unsigned vo = row * (DM * 2) + (cb * 128 + (wave & 1) * 64 + (g & 1) * 16 + (g >> 1) * 8) * 2;
#pragma unroll
  for (int h = 0; h < 2; ++h) {
    const auto s0 = __builtin_amdgcn_permlane16_swap(pk[2 * h][0], pk[2 * h + 1][0], false, false);
    const auto s1 = __builtin_amdgcn_permlane16_swap(pk[2 * h][1], pk[2 * h + 1][1], false, false);
    st16((u32x4){s0[0], s1[0], s0[1], s1[1]}, ro, vo + h * 64, 0);
  }
}

extern "C" void kernel_launch(void* const* d_in, const int* in_sizes, int n_in, void* d_out, int out_size, void* d_ws,
                              size_t ws_size, hipStream_t stream) {
  (void)hipFuncSetAttribute((const void*)ffn_fused, hipFuncAttributeMaxDynamicSharedMemorySize, LDS_BYTES);
  ffn_fused<<<256, 512, LDS_BYTES, stream>>>((const __hip_bfloat16*)d_in[0], (const __hip_bfloat16*)d_in[1],
                                             (const __hip_bfloat16*)d_in[2], (const __hip_bfloat16*)d_in[3], (char*)d_ws,
                                             (__hip_bfloat16*)d_out);
}
```

```cpp
#include <hip/hip_runtime.h>
#include <hip/hip_bf16.h>

typedef __bf16 bf16x8 __attribute__((ext_vector_type(8)));
typedef __bf16 bf16x4 __attribute__((ext_vector_type(4)));
typedef __bf16 bf16x2 __attribute__((ext_vector_type(2)));
typedef float f32x4 __attribute__((ext_vector_type(4)));
typedef float f32x2 __attribute__((ext_vector_type(2)));
typedef unsigned u32x4 __attribute__((ext_vector_type(4)));
typedef unsigned long long u64;
typedef u64 u64x4 __attribute__((ext_vector_type(4)));
typedef u64 u64x2 __attribute__((ext_vector_type(2)));
#define LAS __attribute__((address_space(3)))
#define GAS __attribute__((address_space(1)))
#define DI __device__ __forceinline__
#define SCHED() __builtin_amdgcn_sched_barrier(0)
#define CFENCE() asm volatile("" ::: "memory")
#define BAR() do { CFENCE(); __builtin_amdgcn_s_barrier(); CFENCE(); } while (0)
#define WAIT_VM(n) asm volatile("s_waitcnt vmcnt(%0)" ::"n"(n) : "memory")
#define VIS_LGKM0() __builtin_amdgcn_s_waitcnt(0xC07F)
#define VIS_VM0() __builtin_amdgcn_s_waitcnt(0x0F70)
#define RLX_AGENT __ATOMIC_RELAXED, __HIP_MEMORY_SCOPE_AGENT

constexpr int MT = 1024, DM = 1024, FF = 3584;
constexpr int KS32 = FF / 32;
constexpr unsigned ST1 = 49152;
constexpr unsigned P1B = 16384;
constexpr unsigned SL2 = 16384;
constexpr unsigned WB2 = 4 * SL2;
constexpr unsigned LDS_BYTES = 163840;
constexpr unsigned SPIN_CAP = 1u << 22;
constexpr unsigned CODE_BYTES = 15488;
constexpr int SC1 = 16;
constexpr size_t WS_H = 0;
constexpr size_t WS_PART = (size_t)MT * FF * 2;
constexpr size_t WS_FLAG = WS_PART + (size_t)64 * 4 * 3 * 8192;
constexpr size_t WS_XFLAG = WS_FLAG + 32768;
constexpr size_t WS_EPOCH = WS_XFLAG + 65536;

DI __amdgpu_buffer_rsrc_t mk_rsrc(const void* p, unsigned bytes) {
  return __builtin_amdgcn_make_buffer_rsrc((void*)p, 0, bytes, 0x00020000);
}
template <int AUX, int IMM = 0>
DI void dma16(__amdgpu_buffer_rsrc_t r, unsigned lds_off, unsigned voff, unsigned soff) {
  __builtin_amdgcn_raw_ptr_buffer_load_lds(r, (LAS void*)(size_t)lds_off, 16, voff, soff, IMM, AUX);
}
DI void touch(__amdgpu_buffer_rsrc_t r, unsigned lds_off, unsigned voff, unsigned soff) {
  __builtin_amdgcn_raw_ptr_buffer_load_lds(r, (LAS void*)(size_t)lds_off, 4, voff, soff, 0, 0);
}
DI void st16(u32x4 v, __amdgpu_buffer_rsrc_t r, unsigned voff, unsigned soff) {
  SCHED();
  __builtin_amdgcn_raw_buffer_store_b128(v, r, voff, soff, SC1);
  asm volatile("s_nop 1");
  SCHED();
}
DI bf16x8 lds_b128(unsigned addr) { return *(LAS bf16x8*)(size_t)addr; }
DI bf16x4 lds_tr(unsigned a) { return __builtin_amdgcn_ds_read_tr16_b64_v4bf16((LAS bf16x4*)(size_t)a); }
DI bf16x8 cat(bf16x4 lo, bf16x4 hi) { return __builtin_shufflevector(lo, hi, 0, 1, 2, 3, 4, 5, 6, 7); }
DI f32x4 mfma(bf16x8 a, bf16x8 b, f32x4 c) { return __builtin_amdgcn_mfma_f32_16x16x32_bf16(a, b, c, 0, 0, 0); }
DI unsigned pk_bf16(float lo, float hi) { return __builtin_bit_cast(unsigned, __builtin_convertvector((f32x2){lo, hi}, bf16x2)); }

struct P1C { bf16x8 X[8]; bf16x4 Wl[2][4], Wh[2][4]; f32x4 acc[8][4]; unsigned ax0, ax1, aw0, aw1; };

DI void p1_tile(P1C& c, int& slot) {
  unsigned delta = 0;
#pragma unroll
  for (int k = 0; k < 2; ++k) {
#pragma unroll
    for (int m = 0; m < 8; ++m) {
      if (k == 1 && m == 2) { VIS_LGKM0(); BAR(); SCHED(); }
      const unsigned ax = k ? c.ax0 : c.ax1;
      const int i = m - 2;
      const unsigned aw = ((i & 1) ? c.aw1 : c.aw0) + (i >> 1) * 16384 + (k ? 0 : 8192);
      { const int nn = (m & 1) ? 3 - 0 : 0; c.acc[m][nn] = mfma(cat(c.Wl[k][nn], c.Wh[k][nn]), c.X[m], c.acc[m][nn]); } SCHED();
      if (m >= 1 + k) { c.X[m - 1 - k] = lds_b128(ax + (m - 1 - k) * 2048); SCHED(); }
      if (k == 1 && m == 0) { c.ax1 += delta; asm volatile("" : "+v"(c.ax1)); SCHED(); }
      { const int nn = (m & 1) ? 3 - 1 : 1; c.acc[m][nn] = mfma(cat(c.Wl[k][nn], c.Wh[k][nn]), c.X[m], c.acc[m][nn]); } SCHED();
      if (m >= 2 && m < 6) { c.Wl[1 - k][i & 3] = lds_tr(aw); SCHED(); }
      if (k == 0 && m == 1) {
        asm volatile("" : "+s"(slot));
        delta = slot == 2 ? (unsigned)(-2 * (int)ST1) : ST1;
        asm volatile("" : "+s"(delta));
        SCHED();
      }
      if (k == 0 && m == 6) { c.ax0 += delta; asm volatile("" : "+v"(c.ax0)); SCHED(); }
      if (k == 0 && m == 7) { c.aw1 += delta; asm volatile("" : "+v"(c.aw1)); SCHED(); }
      { const int nn = (m & 1) ? 3 - 2 : 2; c.acc[m][nn] = mfma(cat(c.Wl[k][nn], c.Wh[k][nn]), c.X[m], c.acc[m][nn]); } SCHED();
      if (m >= 2 && m < 6) { c.Wh[1 - k][i & 3] = lds_tr(aw + 1024); SCHED(); }
      if (m == 7 && k == 1) { c.X[6] = lds_b128(ax + 6 * 2048); SCHED(); }
      if (k == 0 && m == 1) { slot = slot == 2 ? 0 : slot + 1; asm volatile("" : "+s"(slot)); SCHED(); }
      if (k == 0 && m == 6) { c.aw0 += delta; asm volatile("" : "+v"(c.aw0)); SCHED(); }
      { const int nn = (m & 1) ? 3 - 3 : 3; c.acc[m][nn] = mfma(cat(c.Wl[k][nn], c.Wh[k][nn]), c.X[m], c.acc[m][nn]); } SCHED();
      if (m == 7) { c.X[7] = lds_b128(ax + 7 * 2048); SCHED(); }
    }
  }
}

struct P2C { bf16x8 H[2][4]; bf16x4 Wl[2][4], Wh[2][4]; f32x4 acc[4][4]; unsigned ah, aw[4]; };

template <int B>
DI void p2_read(P2C& c, int e, unsigned ks) {
  if (e == 2 || e >= 9) { const int m = e == 2 ? 0 : e - 8; c.H[B][m] = lds_b128(c.ah + m * 2048 + ks * 1024); }
  else {
    const int f = e < 2 ? e : e - 1, n = f >> 1;
    if (f & 1) c.Wh[B][n] = lds_tr(c.aw[n] + ks * 8192 + 1024); else c.Wl[B][n] = lds_tr(c.aw[n] + ks * 8192);
  }
}
template <int HF, bool LAST = false>
DI void p1_half(P1C& c, int& slot, unsigned flip, P2C* c2 = nullptr) {
  unsigned delta = 0;
#pragma unroll
  for (int gp = 0; gp < 16; ++gp) {
    const int m = gp >> 1, f = ((gp ^ m) & 1) * 2;
    c.acc[m][HF + f] = mfma(cat(c.Wl[0][f], c.Wh[0][f]), c.X[m], c.acc[m][HF + f]); SCHED();
    if (gp == 0) { asm volatile("" : "+s"(slot)); delta = slot == 2 ? (unsigned)(-2 * (int)ST1) : ST1; asm volatile("" : "+s"(delta)); }
    if (gp >= 2 && !(gp & 1)) c.X[m - 1] = lds_b128(c.ax1 + (m - 1) * 2048);
    if (gp == 1) c.Wl[1][0] = lds_tr(c.aw0 + 8192);
    if (gp == 3) c.Wh[1][0] = lds_tr(c.aw0 + 8192 + 1024);
    if (gp == 5) c.Wl[1][2] = lds_tr(c.aw0 + 16384 + 8192);
    if (gp == 7) c.Wh[1][2] = lds_tr(c.aw0 + 16384 + 8192 + 1024);
    if (gp == 9) { c.aw0 = (c.aw0 + delta) ^ flip; asm volatile("" : "+v"(c.aw0)); }
    if (gp == 11) { c.ax0 += delta; asm volatile("" : "+v"(c.ax0)); }
    if (gp == 13) { slot = slot == 2 ? 0 : slot + 1; asm volatile("" : "+s"(slot)); }
    if (gp == 15) c.X[7] = lds_b128(c.ax1 + 7 * 2048);
    SCHED();
  }
#pragma unroll
  for (int gp = 0; gp < 16; ++gp) {
    const int m = gp >> 1, f = ((gp ^ m) & 1) * 2;
    if (gp == 6) { VIS_LGKM0(); BAR(); SCHED(); }
    c.acc[m][HF + f] = mfma(cat(c.Wl[1][f], c.Wh[1][f]), c.X[m], c.acc[m][HF + f]); SCHED();
    if (LAST) {
      if (gp >= 6) p2_read<0>(*c2, gp - 6, 0);
      if (gp >= 14) p2_read<0>(*c2, gp - 4, 0);
      SCHED();
      continue;
    }
    if (gp == 0) { c.ax1 += delta; asm volatile("" : "+v"(c.ax1)); }
    if (gp == 6) { c.Wl[0][0] = lds_tr(c.aw0); c.Wh[0][0] = lds_tr(c.aw0 + 1024); }
    if (gp == 7) c.X[0] = lds_b128(c.ax0);
    if (gp == 8) c.Wl[0][2] = lds_tr(c.aw0 + 16384);
    if (gp == 9) c.Wh[0][2] = lds_tr(c.aw0 + 16384 + 1024);
    if (gp >= 10) c.X[gp - 9] = lds_b128(c.ax0 + (gp - 9) * 2048);
    if (gp == 15) c.X[7] = lds_b128(c.ax0 + 7 * 2048);
    SCHED();
  }
}

template <int HF>
DI void p1_epi(P1C& c, unsigned la0, unsigned la2, __amdgpu_buffer_rsrc_t rh, unsigned vo0) {
  u32x4 o[4];
  f32x4 ta = c.acc[0][HF] * -1.44269504088896f, tb = c.acc[1][HF] * -1.44269504088896f;
  SCHED();
#pragma unroll
  for (int m = 0; m < 8; m += 2) {
#pragma unroll
    for (int i = 0; i < 4; ++i) ta[i] = __builtin_amdgcn_exp2f(ta[i]);
#pragma unroll
    for (int i = 0; i < 4; ++i) tb[i] = __builtin_amdgcn_exp2f(tb[i]);
    SCHED();
    f32x4 da = ta + 1.0f, db = tb + 1.0f;
    SCHED();
#pragma unroll
    for (int i = 0; i < 4; ++i) da[i] = __builtin_amdgcn_rcpf(da[i]);
#pragma unroll
    for (int i = 0; i < 4; ++i) db[i] = __builtin_amdgcn_rcpf(db[i]);
    SCHED();
    if (m < 6) { ta = c.acc[m + 2][HF] * -1.44269504088896f; tb = c.acc[m + 3][HF] * -1.44269504088896f; }
    unsigned vo = vo0 + m * (KS32 * 1024);
    asm volatile("" : "+v"(vo));
    SCHED();
    da *= c.acc[m][HF]; db *= c.acc[m + 1][HF];
    SCHED();
    da *= c.acc[m][HF + 2]; db *= c.acc[m + 1][HF + 2];
    SCHED();
    const auto s0 = __builtin_amdgcn_permlane16_swap(pk_bf16(da[0], da[1]), pk_bf16(db[0], db[1]), false, false);
    const auto s1 = __builtin_amdgcn_permlane16_swap(pk_bf16(da[2], da[3]), pk_bf16(db[2], db[3]), false, false);
    o[m >> 1] = (u32x4){s0[0], s1[0], s0[1], s1[1]};
    SCHED();
    __builtin_amdgcn_raw_buffer_store_b128(o[m >> 1], rh, vo, 0, SC1);
    *(LAS u32x4*)(size_t)(((m & 2) ? la2 : la0) + (m >> 2) * 8192) = o[m >> 1];
    SCHED();
  }
#pragma unroll
  for (int m = 0; m < 4; ++m) asm volatile("" ::"v"(o[m]));
}

template <bool FIRST>
DI void p2_tile(P2C& c, int& sw6, int& sh4) {
  unsigned dw = 0, dh = 0;
#pragma unroll
  for (int gp = 0; gp < 16; ++gp) {
    const int m = gp >> 2, n = (m & 1) ? 3 - (gp & 3) : (gp & 3);
    c.acc[m][n] = mfma(cat(c.Wl[0][n], c.Wh[0][n]), c.H[0][m], FIRST ? (f32x4){0.f, 0.f, 0.f, 0.f} : c.acc[m][n]); SCHED();
    if (gp < 12) { p2_read<1>(c, gp, 1); SCHED(); }
    if (gp == 12) { asm volatile("" : "+s"(sw6)); dw = sw6 == 5 ? (unsigned)(-5 * (int)SL2) : SL2; asm volatile("" : "+s"(dw)); SCHED(); }
    if (gp == 13) { asm volatile("" : "+s"(sh4)); dh = sh4 == 3 ? (unsigned)(-3 * (int)SL2) : SL2; asm volatile("" : "+s"(dh)); SCHED(); }
    if (gp == 14) { c.aw[0] += dw; c.aw[1] += dw; asm volatile("" : "+v"(c.aw[0]), "+v"(c.aw[1])); SCHED(); }
    if (gp == 15) { c.aw[2] += dw; c.aw[3] += dw; asm volatile("" : "+v"(c.aw[2]), "+v"(c.aw[3])); SCHED(); }
  }
#pragma unroll
  for (int gp = 0; gp < 16; ++gp) {
    const int m = gp >> 2, n = (m & 1) ? 3 - (gp & 3) : (gp & 3);
    if (gp == 4) { VIS_LGKM0(); BAR(); SCHED(); }
    c.acc[m][n] = mfma(cat(c.Wl[1][n], c.Wh[1][n]), c.H[1][m], c.acc[m][n]); SCHED();
    if (gp == 0) { c.ah += dh; asm volatile("" : "+v"(c.ah)); SCHED(); }
    if (gp == 1) { sw6 = sw6 == 5 ? 0 : sw6 + 1; asm volatile("" : "+s"(sw6)); SCHED(); }
    if (gp == 2) { sh4 = (sh4 + 1) & 3; asm volatile("" : "+s"(sh4)); SCHED(); }
    if (gp >= 4) { p2_read<0>(c, gp - 4, 0); SCHED(); }
  }
}

template <int B>
DI void p2_fread(P2C& c, int e, unsigned ks) {
  if (e == 2 || e >= 9) { const int m = e == 2 ? 1 : e - 7; c.H[B][m] = lds_b128(c.ah + m * 2048 + ks * 1024); }
  else p2_read<B>(c, e, ks);
}
DI void p2_ftile(P2C& c, int& sw6, int& sh4) {
  unsigned dw = 0, dh = 0;
#pragma unroll
  for (int gp = 0; gp < 12; ++gp) {
    const int m = 1 + (gp >> 2), n = (m & 1) ? (gp & 3) : 3 - (gp & 3);
    c.acc[m][n] = mfma(cat(c.Wl[0][n], c.Wh[0][n]), c.H[0][m], c.acc[m][n]); SCHED();
    if (gp < 11) p2_fread<1>(c, gp, 1);
    if (gp == 5) { asm volatile("" : "+s"(sw6)); dw = sw6 == 5 ? (unsigned)(-5 * (int)SL2) : SL2; asm volatile("" : "+s"(dw)); }
    if (gp == 6) { asm volatile("" : "+s"(sh4)); dh = sh4 == 3 ? (unsigned)(-3 * (int)SL2) : SL2; asm volatile("" : "+s"(dh)); }
    if (gp == 7) { c.aw[0] += dw; asm volatile("" : "+v"(c.aw[0])); }
    if (gp == 9) { c.aw[1] += dw; asm volatile("" : "+v"(c.aw[1])); }
    if (gp == 10) { c.aw[2] += dw; asm volatile("" : "+v"(c.aw[2])); }
    if (gp == 11) { c.aw[3] += dw; c.ah += dh; asm volatile("" : "+v"(c.aw[3]), "+v"(c.ah)); }
    SCHED();
  }
  BAR();
  SCHED();
#pragma unroll
  for (int gp = 0; gp < 12; ++gp) {
    const int m = 1 + (gp >> 2), n = (m & 1) ? (gp & 3) : 3 - (gp & 3);
    c.acc[m][n] = mfma(cat(c.Wl[1][n], c.Wh[1][n]), c.H[1][m], c.acc[m][n]); SCHED();
    if (gp < 11) p2_fread<0>(c, gp, 0);
    if (gp == 11) { sw6 = sw6 == 5 ? 0 : sw6 + 1; sh4 = (sh4 + 1) & 3; asm volatile("" : "+s"(sw6), "+s"(sh4)); }
    SCHED();
  }
}
DI void p2_otile(P2C& c, unsigned dw, unsigned dh) {
#pragma unroll
  for (int e = 0; e < 9; ++e) p2_read<0>(c, e, 0);
#pragma unroll
  for (int e = 0; e < 9; ++e) p2_read<1>(c, e, 1);
#pragma unroll
  for (int k = 0; k < 2; ++k)
#pragma unroll
    for (int n = 0; n < 4; ++n) c.acc[0][n] = mfma(cat(c.Wl[k][n], c.Wh[k][n]), c.H[k][0], c.acc[0][n]);
  c.ah += dh;
#pragma unroll
  for (int n = 0; n < 4; ++n) c.aw[n] += dw;
}

__global__ __launch_bounds__(512, 2) void ffn_fused(const __hip_bfloat16*, const __hip_bfloat16*, const __hip_bfloat16*,
                                                    const __hip_bfloat16*, char*, __hip_bfloat16*) {
  extern __shared__ __attribute__((aligned(16))) char smem[];
  u64x4 ka; u64x2 kb;
  asm volatile("s_load_dwordx8 %0, %2, 0x0\n\ts_load_dwordx4 %1, %2, 0x20" : "=&s"(ka), "=&s"(kb) : "s"(__builtin_amdgcn_kernarg_segment_ptr()));
  u64 pc0; asm volatile("s_getpc_b64 %0" : "=s"(pc0));
  const int bid = blockIdx.x, xcd = bid & 7, idx = bid >> 3;
  const int ksp = xcd >> 1, xe = xcd & 1, tb = idx >> 2, u = idx & 3;
  const bool spare = u == 3 && (tb >> 2) != xe;
  const int j = u < 3 ? xe * 4 + u : 3;
  const int fb = ksp * 7 + j, cb = xe * 4 + u;
  const int wave = __builtin_amdgcn_readfirstlane(threadIdx.x >> 6), l = threadIdx.x & 63;
  const unsigned L0 = (unsigned)(size_t)(LAS char*)smem;
  if (wave == 3) touch(mk_rsrc((const void*)(pc0 & ~(u64)127), CODE_BYTES), L0 + 8192 + 768, (idx + (l >> 4) * 32) * 128, 0);
  unsigned vx0, vw0, vp0;
  {
    const int xr = 8 * wave + (l >> 3);
    const unsigned sx = ((xr >> 1) & 3) | ((((xr >> 2) ^ (xr >> 3)) & 1) << 2);
    vx0 = xr * 2048 + (((l & 7) ^ sx) << 4);
    const unsigned sw = (l >> 4) | ((wave & 2) << 1);
    vw0 = (4 * wave + (l >> 4)) * (FF * 2) + (((((l & 15) >> 1) ^ sw)) << 5) + ((l & 1) << 4);
    vp0 = wave ? (idx * 32 + (l >> 1)) * 2048 + (l & 1) * 128
               : ((idx & 15) * 8 + (l >> 3)) * (FF * 2) + (ksp * 7 + xe * 3) * 256 + (l & 7) * 128;
  }
  if (spare) {
    vw0 = ((((tb & 1) * 4 + (wave & 3)) * 8 + (l >> 3)) + (2 + (wave >> 2)) * 64) * (FF * 2) + (ksp * 7 + xe * 3) * 256 + (l & 7) * 128;
    vx0 = (xcd * 128 + (tb & 3) * 32 + (l & 31)) * 2048 + ((wave - 1) * 2 + (l >> 5)) * 128;
  }
  asm volatile("s_waitcnt lgkmcnt(0)" : "+s"(ka), "+s"(kb), "+v"(vx0), "+v"(vw0), "+v"(vp0));
  const void *x = (const void*)ka.x, *wg = (const void*)ka.y, *wu = (const void*)ka.z, *wd = (const void*)ka.w;
  char* ws = (char*)kb.x;
  void* out = (void*)kb.y;
  u64 tag = __hip_atomic_load((GAS u64*)(ws + WS_EPOCH), RLX_AGENT);
  CFENCE();
  GAS u64* hflag = (GAS u64*)(ws + WS_FLAG) + (tb * 4 + ksp) * 128;
  auto unit_table = [&]() {
    const u64 a = (0xCA86420CA86420ull >> (4 * j)) & 0xFFFFFFFull, b = a | 0x1111111ull;
    return spare ? (a | (b << 28)) : ((a & 15ull) | ((b & 15ull) << 4) | ((a >> 4) << 8) | ((b >> 4) << 32));
  };
  const __amdgpu_buffer_rsrc_t rx = mk_rsrc(x, MT * DM * 2), rg = mk_rsrc(wg, DM * FF * 2), ru = mk_rsrc(wu, DM * FF * 2);
  if (wave < 2) touch(wave ? rx : ((idx & 16) ? ru : rg), L0 + 8192 + wave * 256, vp0, 0);
  if (spare) {
    if (wave == 2 || wave == 3) touch(rx, L0 + wave * 256, vx0, 0);
    touch((tb & 2) ? ru : rg, L0 + wave * 256, vw0, 0);
  }
  CFENCE();
  if (__builtin_expect(!spare, 1)) {
    const unsigned sx0 = tb * 128 * 2048, sw0 = fb * 256, lb = L0 + P1B + wave * 1024;
#pragma unroll
    for (int a = 0; a < 2; ++a) {
      dma16<0>(rg, lb + 16384 + a * 8192, vw0, sw0 + a * 32 * (FF * 2));
      dma16<0>(ru, lb + 32768 + a * 8192, vw0, sw0 + a * 32 * (FF * 2));
      dma16<0>(rx, lb + a * 8192, vx0, sx0 + a * 64 * 2048);
    }
    if (wave >= 4) {
#pragma unroll
      for (int a = 0; a < 2; ++a) {
        dma16<0>(rg, lb + ST1 + 16384 + (a - 1) * 4096, vw0, sw0 + (64 + (a - 1) * 16) * (FF * 2));
        dma16<0>(ru, lb + ST1 + 32768 + (a - 1) * 4096, vw0, sw0 + (64 + (a - 1) * 16) * (FF * 2));
        dma16<0>(rx, lb + ST1 + (a - 1) * 4096, vx0 - 32 * 2048, sx0 + a * 32 * 2048 + 128);
      }
    }
  } else {
    const u64 t_start = __builtin_amdgcn_s_memrealtime();
    const int part = (tb & 3) * 4 + (wave & 3);
    const __amdgpu_buffer_rsrc_t rs = (part & 8) ? ru : rg;
    const unsigned vo = ((part & 7) * 8 + (l >> 3)) * (FF * 2) + (ksp * 7 + xe * 3) * 256 + (l & 7) * 128;
#pragma unroll 1
    for (int t = 4 + (wave >> 2), it = 3; t < 16; t += 2, ++it) {
      touch(rs, L0 + wave * 256, vo, t * 64 * (FF * 2));
      if (it == wave)
        touch(rx, L0 + wave * 256, (xcd * 128 + (tb & 3) * 32 + (l & 31)) * 2048 + (wave * 2 + (l >> 5)) * 128, 0);
      WAIT_VM(1);
    }
    if (wave < 4 || wave >= 6) {
      const __amdgpu_buffer_rsrc_t rw = mk_rsrc(wd, FF * DM * 2);
      const unsigned vw = (ksp * 896 + (l >> 3)) * 2048 + xe * 1024 + (l & 7) * 128;
#pragma unroll 1
      for (int e = (tb & 3) * 6 + (wave < 4 ? wave : wave - 2); e < 112; e += 24) {
        if (__builtin_amdgcn_s_memrealtime() - t_start > 1000) break;
        const int un = e >> 3, r = (un < 7 ? 2 * un : 2 * un - 13) * 8 + (e & 7);
        touch(rw, L0 + wave * 256, vw, r * 8 * 2048);
        WAIT_VM(1);
      }
    }
    VIS_VM0();
    CFENCE();
  }

  if (wave >= 6) {
    const int lw = wave - 6;
    if (!spare) { WAIT_VM(6); BAR(); }
    asm volatile("" : "+v"(tag));
    tag += 1ull;
    asm volatile("" : "+v"(tag));
    WAIT_VM(0);
    unsigned w0 = 0, w1 = 0;
    const u64 tbl = unit_table();
    GAS u64* fw;
    { const unsigned un = (unsigned)(tbl >> (4 * ((l >> 2) < 14 ? (l >> 2) : 13))) & 15u; fw = hflag + (un >> 1) * 16 + (un & 1) * 4 + (l & 3); }
    if (!spare) {
#pragma unroll 1
      for (int t = 1; t < 15; ++t) BAR();
      const __amdgpu_buffer_rsrc_t rw = mk_rsrc(wd, FF * DM * 2);
      const int me = idx - ((xe ? (tb < 4 ? tb : 4) : (tb > 4 ? tb - 4 : 0)));
      const unsigned vb = (ksp * 896 + me * 32 + lw * 16 + (l >> 3)) * 2048 + xe * 1024 + (l & 7) * 128;
      w0 = __builtin_amdgcn_raw_buffer_load_b32(rw, vb, 0, 0);
      w1 = __builtin_amdgcn_raw_buffer_load_b32(rw, vb, 8 * 2048, 0);
      BAR();
    }
    BAR();
    const __amdgpu_buffer_rsrc_t rh = mk_rsrc(ws + WS_H, MT * FF * 2);
    unsigned tofs[4];
#pragma unroll
    for (int a = 0; a < 4; ++a) tofs[a] = (unsigned)((tb * 8 + lw * 4 + ((a + ksp) & 3)) * KS32 + ksp * 28) * 1024u;
    auto issue_h = [&](int kt) {
      const unsigned sb = ((unsigned)(tbl >> (4 * kt)) & 15u) * 2048u;
      const unsigned lb = L0 + (kt & 3) * SL2 + lw * 8192;
#pragma unroll
      for (int a = 0; a < 4; ++a) {
        dma16<SC1>(rh, lb + a * 2048, l * 16, sb + tofs[a]);
        dma16<SC1, 1024>(rh, lb + a * 2048, l * 16, sb + tofs[a]);
      }
    };
    int nrdy = 0;
    auto look = [&](u64 v) { nrdy = __builtin_ctzll(~(__ballot(v == tag) | (spare ? 0ull : 255ull)) | (1ull << 63)) >> 2; CFENCE(); };
    if (spare) {
      if (l == 0) __hip_atomic_store(hflag + 7 * 16 + lw, tag, RLX_AGENT);
#pragma unroll 1
      for (unsigned spins = 0; spins < SPIN_CAP && nrdy < 1; ++spins) { __builtin_amdgcn_s_sleep(8); look(__hip_atomic_load(fw, RLX_AGENT)); }
    } else {
      BAR(); BAR(); BAR();
#pragma unroll 1
      for (int i = 0; i < 3 && nrdy < 4; ++i) look(__hip_atomic_load(fw, RLX_AGENT));
    }
    VIS_VM0();
    asm volatile("" ::"v"(w0), "v"(w1));
    int nx = spare ? 0 : 2;
#pragma unroll 1
    for (int kt = spare ? 0 : 1; kt < 14; ++kt) {
      if (nx == kt + 2) {
        const int fe = nrdy - 3;
#pragma unroll 1
        for (; kt < fe; ++kt) { issue_h(kt + 2); WAIT_VM(16); BAR(); }
        nx = kt + 2;
      }
      const int lim = kt < 11 ? (kt ? kt + 2 : 3) : 13;
      if (__builtin_expect(kt >= nrdy, 0)) {
#pragma unroll 1
        for (unsigned spins = 0; spins < SPIN_CAP && kt >= nrdy; ++spins) look(__hip_atomic_load(fw, RLX_AGENT));
      }
      u64 pv;
      const bool pend = lim + 1 >= nrdy;
      if (__builtin_expect(pend, 0)) asm volatile("global_load_dwordx2 %0, %1, off sc1" : "=v"(pv) : "v"(fw) : "memory");
      const int n0 = nx;
#pragma unroll 1
      while (nx <= lim && nx < nrdy) { issue_h(nx); ++nx; }
      if (__builtin_expect(nx >= kt + 3, 1)) WAIT_VM(16); else if (nx == kt + 2) WAIT_VM(8); else WAIT_VM(0);
      BAR();
      if (__builtin_expect(pend, 0)) {
        if (nx > n0) WAIT_VM(8); else WAIT_VM(0);
        asm volatile("" : "+v"(pv));
        look(pv);
      }
    }
    BAR();
    if (bid == 0 && lw == 0) {
      GAS u64* fa = (GAS u64*)(ws + WS_FLAG);
#pragma unroll 1
      for (unsigned spins = 0; spins < SPIN_CAP; ++spins) {
        bool ok = true;
#pragma unroll
        for (int i = 0; i < 4; ++i) ok &= __hip_atomic_load(fa + (i * 64 + l) * 16, RLX_AGENT) == tag;
        if (__all(ok)) break;
        __builtin_amdgcn_s_sleep(2);
      }
      if (l == 0) __hip_atomic_store((GAS u64*)(ws + WS_EPOCH), tag, RLX_AGENT);
    }
    return;
  }
  if (wave >= 4) {
    const int lw = wave - 4;
    const __amdgpu_buffer_rsrc_t rw = mk_rsrc(wd, FF * DM * 2);
    const unsigned vd = (8 * lw + (l >> 4)) * (DM * 2) + (((((l & 15) >> 1) ^ ((l >> 4) | (lw << 2)))) << 5) + ((l & 1) << 4);
    const unsigned sd0 = cb * 256 + ksp * 896 * (DM * 2);
    const unsigned vd2 = vd + 4 * (DM * 2) - 1024;
    u64 tbl = 0;
    auto issue_w = [&](int kt, unsigned slot) {
      const unsigned sb = sd0 + ((unsigned)(tbl >> (4 * kt)) & 15u) * (64 * DM * 2);
      const unsigned lb = L0 + WB2 + slot * SL2 + lw * 2048;
#pragma unroll
      for (int a = 0; a < 4; ++a) {
        dma16<0>(rw, lb + a * 4096, vd, sb + a * 16 * (DM * 2));
        dma16<0, 1024>(rw, lb + a * 4096, vd2, sb + a * 16 * (DM * 2));
      }
    };
    if (!spare) {
      const int xr = 8 * lw + (l >> 3);
      const unsigned sx = ((xr >> 1) & 3) | ((((xr >> 2) ^ (xr >> 3)) & 1) << 2);
      const unsigned vx = xr * 2048 + (((l & 7) ^ sx) << 4);
      const unsigned sw = (l >> 4) | (lw << 2);
      const unsigned vw = (8 * lw + (l >> 4)) * (FF * 2) + (((((l & 15) >> 1) ^ sw)) << 5) + ((l & 1) << 4);
      const unsigned vw2 = vw + 4 * (FF * 2) - 1024, vx2 = vx + 32768 - 2048;
      unsigned lb = L0 + P1B + ST1 + 8192, so = fb * 256 + 96 * (FF * 2), sxo = tb * 128 * 2048 + 128 + 131072;
      int n = 3;
#pragma unroll 1
      for (int t = 0; t < 14; ++t) {
        if (t == 0) WAIT_VM(6); else if (t == 1) WAIT_VM(12); else WAIT_VM(24);
        BAR();
        const int lim = t ? 2 * t + 6 : 5;
#pragma unroll 1
        for (; n < lim; ++n) {
#pragma unroll
          for (int a = 0; a < 2; ++a) {
            dma16<0>(rg, lb + 16384 + a * 4096 + lw * 2048, vw, so + a * 16 * (FF * 2));
            dma16<0, 1024>(rg, lb + 16384 + a * 4096 + lw * 2048, vw2, so + a * 16 * (FF * 2));
            dma16<0>(ru, lb + 32768 + a * 4096 + lw * 2048, vw, so + a * 16 * (FF * 2));
            dma16<0, 1024>(ru, lb + 32768 + a * 4096 + lw * 2048, vw2, so + a * 16 * (FF * 2));
            dma16<0>(rx, lb + a * 4096 + lw * 1024, vx, sxo + a * 65536);
            dma16<0, 2048>(rx, lb + a * 4096 + lw * 1024, vx2, sxo + a * 65536);
          }
          so += 32 * (FF * 2);
          if (n & 1) { lb += ST1 - 8192; if (lb == L0 + P1B + 3 * ST1) lb = L0 + P1B; sxo += 128 - 131072; }
          else { lb += 8192; sxo += 131072; }
        }
      }
      WAIT_VM(24); BAR();
      WAIT_VM(0); BAR();
      tbl = unit_table();
      BAR();
      BAR();
      issue_w(0, 0);
      BAR();
      issue_w(1, 1);
      WAIT_VM(8); BAR();
#pragma unroll 1
      for (int kt = 2; kt < 4; ++kt) issue_w(kt, kt);
      WAIT_VM(16); BAR();
#pragma unroll 1
      for (int kt = 4; kt < 6; ++kt) issue_w(kt, kt);
      WAIT_VM(24); BAR();
      issue_w(6, 0);
    } else {
      tbl = unit_table();
#pragma unroll 1
      for (int kt = 0; kt < 6; ++kt) issue_w(kt, kt);
      BAR();
      WAIT_VM(0); BAR();
      BAR();
      BAR();
      issue_w(6, 0);
    }
    unsigned slot = 1;
#pragma unroll 1
    for (int kt = 3; kt < 10; ++kt) {
      WAIT_VM(24); BAR();
      issue_w(kt + 4, slot);
      slot = slot == 5 ? 0 : slot + 1;
    }
    WAIT_VM(24); BAR();
    WAIT_VM(16); BAR();
    WAIT_VM(8); BAR();
    WAIT_VM(0); BAR();
    BAR();
    return;
  }

  P1C c1;
  P2C c;
  unsigned la0, la2, vo0;
  {
    const int g = l >> 4, q = (l & 15) >> 2, p = l & 3, r15 = l & 15;
    const unsigned b = L0 + P1B;
    const unsigned sx = ((r15 >> 1) & 3) | ((((r15 >> 2) ^ (r15 >> 3)) & 1) << 2);
    c1.ax0 = b + r15 * 128 + ((g ^ sx) << 4);
    c1.ax1 = b + r15 * 128 + (((4 + g) ^ sx) << 4);
    const unsigned sw = q | ((g & 1) << 2);
    c1.aw0 = b + 16384 + (8 * g + q) * 256 + p * 8 + ((wave ^ sw) << 5);
    c1.aw1 = b + 16384 + (8 * g + q) * 256 + p * 8 + (((4 + wave) ^ sw) << 5);
    asm volatile("" : "+v"(c1.ax0), "+v"(c1.ax1), "+v"(c1.aw0), "+v"(c1.aw1));
    c.ah = L0 + (wave >> 1) * 8192 + l * 16;
#pragma unroll
    for (int n = 0; n < 4; ++n) c.aw[n] = L0 + WB2 + (8 * g + q) * 256 + p * 8 + (((4 * (wave & 1) + n) ^ sw) << 5);
    const unsigned g1 = g & 1, in = ((wave & 1) * 2 + (g >> 1)) * 256 + r15 * 16 + (wave >> 1) * 1024;
    la0 = L0 + ((g1 - ksp) & 3) * 2048 + in; la2 = L0 + ((g1 + 2 - ksp) & 3) * 2048 + in;
    vo0 = (unsigned)((tb * 8 + g1) * KS32 + fb * 4 + 2) * 1024u + in;
    asm volatile("" : "+v"(c.ah), "+v"(c.aw[0]), "+v"(c.aw[1]), "+v"(c.aw[2]), "+v"(c.aw[3]), "+v"(la0), "+v"(la2), "+v"(vo0));
  }
#pragma unroll
  for (int m = 0; m < 8; ++m)
#pragma unroll
    for (int n = 0; n < 4; ++n) { c1.acc[m][n] = (f32x4){0.f, 0.f, 0.f, 0.f}; asm volatile("" : "+v"(c1.acc[m][n])); }
  SCHED();
  asm volatile("" : "+v"(tag));
  tag += 1ull;
  tag = ((u64)(unsigned)__builtin_amdgcn_readfirstlane((unsigned)(tag >> 32)) << 32) | (unsigned)__builtin_amdgcn_readfirstlane((unsigned)tag);
  asm volatile("" : "+s"(tag));
  __builtin_amdgcn_s_waitcnt(0x0070);
  SCHED();
  if (!spare) {
    BAR();
#pragma unroll
    for (int i = 0; i < 4; ++i) {
      const unsigned a = ((i & 1) ? c1.aw1 : c1.aw0) + (i >> 1) * 16384;
      c1.Wl[0][i] = lds_tr(a); c1.Wh[0][i] = lds_tr(a + 1024);
      if (i == 0) c1.X[0] = lds_b128(c1.ax0);
    }
#pragma unroll
    for (int m = 1; m < 8; ++m) c1.X[m] = lds_b128(c1.ax0 + m * 2048);
    SCHED();
    int slot = 0;
#pragma unroll 1
    for (int t = 0; t < 13; ++t) p1_tile(c1, slot);
#pragma unroll 1
    for (int t = 0; t < 3; ++t) p1_half<0>(c1, slot, t == 2 ? 128u : 0u);
    const __amdgpu_buffer_rsrc_t rh = mk_rsrc(ws + WS_H, MT * FF * 2);
    p1_epi<0>(c1, la0, la2, rh, vo0 - 2048);
#pragma unroll 1
    for (int t = 0; t < 2; ++t) {
      p1_half<1>(c1, slot, 0u);
      if (t == 0) { WAIT_VM(0); if (l == 0) __hip_atomic_store(hflag + j * 16 + wave, tag, RLX_AGENT); }
    }
    p1_half<1, true>(c1, slot, 0u, &c);
    p1_epi<1>(c1, la0 + SL2, la2 + SL2, rh, vo0);
  } else {
    BAR();
    BAR();
#pragma unroll
    for (int e = 0; e < 12; ++e) p2_read<0>(c, e, 0);
    SCHED();
  }

  int tz = threadIdx.x; asm volatile("" : "+v"(tz));
  const int ln = tz & 63;
  int sw6 = 0, sh4 = 0;
  p2_tile<true>(c, sw6, sh4);
  if (!spare) {
    WAIT_VM(0);
    if (ln == 0) __hip_atomic_store(hflag + j * 16 + 4 + wave, tag, RLX_AGENT);
  }
#pragma unroll 1
  for (int kt = 1; kt < 10; ++kt) p2_tile<false>(c, sw6, sh4);
#pragma unroll 1
  for (int kt = 10; kt < 14; ++kt) p2_ftile(c, sw6, sh4);
  const int tile = tb * 8 + cb;
  const __amdgpu_buffer_rsrc_t rp = mk_rsrc(ws + WS_PART, 64u * 4 * 3 * 8192);
  GAS u64* f = (GAS u64*)(ws + WS_XFLAG) + ((tile * 4 + ksp) * 4 + wave) * 8 + (ln < 3 ? ln : 0);
#pragma unroll
  for (int mm = 1; mm < 4; ++mm) {
    const unsigned so = (unsigned)(((tile * 4 + ((ksp + mm) & 3)) * 3 + (mm - 1)) * 4 + wave) * 2048u;
#pragma unroll
    for (int n = 0; n < 4; n += 2) {
      const f32x4 a = c.acc[mm][n], b = c.acc[mm][n + 1];
      st16((u32x4){pk_bf16(a[0], a[1]), pk_bf16(a[2], a[3]), pk_bf16(b[0], b[1]), pk_bf16(b[2], b[3])}, rp, ln * 16 + n * 512, so);
    }
  }
  u64 fl = 0;
#pragma unroll
  for (int n = 0; n < 4; ++n) c.aw[n] += 2 * SL2;
#pragma unroll 1
  for (int kt = 10; kt < 14; ++kt) {
    if (kt == 12) {
      WAIT_VM(0);
      typedef unsigned u32x2 __attribute__((ext_vector_type(2)));
      const unsigned fo = ln < 3 ? (unsigned)(((tile * 4 + ((ksp + ln + 1) & 3)) * 4 + wave) * 8 + ln) * 8u : 0x80000000u;
      __builtin_amdgcn_raw_buffer_store_b64((u32x2){(unsigned)tag, (unsigned)(tag >> 32)}, mk_rsrc(ws + WS_XFLAG, 65536), fo, 0, SC1);
      fl = __hip_atomic_load(f, RLX_AGENT);
    }
    p2_otile(c, kt == 11 ? (unsigned)(-5 * (int)SL2) : SL2, kt == 11 ? (unsigned)(-3 * (int)SL2) : SL2);
  }
  SCHED();
  asm volatile("" : "+v"(fl));
  unsigned seen = (unsigned)__ballot(fl == tag);
  asm volatile("" : "+s"(seen));
  u32x4 px[3][2];
  unsigned got = 0;
#pragma unroll 1
  for (unsigned spins = 0; spins < SPIN_CAP; ++spins) {
    const unsigned now = seen & 7u & ~got;
    CFENCE();
#pragma unroll
    for (int s = 0; s < 3; ++s)
      if (now & (1u << s)) {
        const unsigned so = (unsigned)(((tile * 4 + ksp) * 3 + s) * 4 + wave) * 2048u;
#pragma unroll
        for (int n = 0; n < 2; ++n) px[s][n] = __builtin_amdgcn_raw_buffer_load_b128(rp, ln * 16 + n * 1024, so, SC1);
      }
    got |= now;
    if (got == 7u) break;
    seen = (unsigned)__ballot(__hip_atomic_load(f, RLX_AGENT) == tag);
    asm volatile("" : "+s"(seen));
  }
  unsigned pk[4][2];
#pragma unroll
  for (int n = 0; n < 4; ++n) {
    f32x4 v = c.acc[0][n];
#pragma unroll
    for (int s = 0; s < 3; ++s) {
      const unsigned lo = px[s][n >> 1][(n & 1) * 2], hi = px[s][n >> 1][(n & 1) * 2 + 1];
      v += __builtin_bit_cast(f32x4, (u32x4){lo << 16, lo & 0xFFFF0000u, hi << 16, hi & 0xFFFF0000u});
    }
    pk[n][0] = pk_bf16(v[0], v[1]);
    pk[n][1] = pk_bf16(v[2], v[3]);
  }
  const __amdgpu_buffer_rsrc_t ro = mk_rsrc(out, MT * DM * 2);
  const unsigned g = ln >> 4, row = tb * 128 + (wave >> 1) * 64 + ksp * 16 + (ln & 15);
  const unsigned vo = row * (DM * 2) + (cb * 128 + (wave & 1) * 64 + (g & 1) * 16 + (g >> 1) * 8) * 2;
#pragma unroll
  for (int h = 0; h < 2; ++h) {
    const auto s0 = __builtin_amdgcn_permlane16_swap(pk[2 * h][0], pk[2 * h + 1][0], false, false);
    const auto s1 = __builtin_amdgcn_permlane16_swap(pk[2 * h][1], pk[2 * h + 1][1], false, false);
    st16((u32x4){s0[0], s1[0], s0[1], s1[1]}, ro, vo + h * 64, 0);
  }
}

extern "C" void kernel_launch(void* const* d_in, const int* in_sizes, int n_in, void* d_out, int out_size, void* d_ws,
                              size_t ws_size, hipStream_t stream) {
  (void)hipFuncSetAttribute((const void*)ffn_fused, hipFuncAttributeMaxDynamicSharedMemorySize, LDS_BYTES);
  ffn_fused<<<256, 512, LDS_BYTES, stream>>>((const __hip_bfloat16*)d_in[0], (const __hip_bfloat16*)d_in[1],
                                             (const __hip_bfloat16*)d_in[2], (const __hip_bfloat16*)d_in[3], (char*)d_ws,
                                             (__hip_bfloat16*)d_out);
}
```
